```python
import torch

def _softmax(x, axis=-1):
    return torch.softmax(x, dim=axis)
def _gelu(x):
    return torch.nn.functional.gelu(x, approximate="tanh")
def _silu(x):
    return torch.nn.functional.silu(x)
def _sigmoid(x):
    return torch.sigmoid(x)
def _layernorm(x, axis=-1, eps=1e-5):
    m = x.mean(dim=axis, keepdim=True)
    v = x.var(dim=axis, keepdim=True, unbiased=False)
    return (x - m) / torch.sqrt(v + eps)
def _scatter_add(tgt, idx, src, axis=0):
    return tgt.clone().index_add_(axis, idx, src)
def _segment_sum(src, seg, n):
    out = torch.zeros((n,) + src.shape[1:], dtype=src.dtype)
    return out.index_add_(0, seg, src)
def _pad(x, pw):
    flat = tuple(v for pair in reversed(pw) for v in pair)
    return torch.nn.functional.pad(x, flat)

kernel_name = 'parallel_sum__ssm_conv_s4__swiglu_ffn__prompt'

def setup_inputs():

    _w = (lambda a: (lambda t: t.to(torch.bfloat16) if t.is_floating_point() else t)(torch.as_tensor(a)))
    def _A_setup():
        B, L, D, N = 1, 2048, 512, 128
        A_re = -(lambda _x: torch.exp(torch.as_tensor(_x)))((lambda *s: torch.randn(*s, dtype=torch.bfloat16) * 3.2)(D, N))
        A_im = torch.pi * (lambda *s: torch.randn(*s, dtype=torch.bfloat16) * 0.1)(D, N)
        B_re = (lambda *s: torch.randn(*s, dtype=torch.bfloat16) * 0.4)(D, N)
        B_im = (lambda *s: torch.randn(*s, dtype=torch.bfloat16) * 0.4)(D, N)
        C_re = (lambda *s: torch.randn(*s, dtype=torch.bfloat16) * 0.4)(D, N)
        C_im = (lambda *s: torch.randn(*s, dtype=torch.bfloat16) * 0.4)(D, N)
        log_dt = (lambda *s: torch.randn(*s, dtype=torch.bfloat16) * 0.1)(D) * 0.5 - 4.0
        Dskip = (lambda *s: torch.randn(*s, dtype=torch.bfloat16) * 0.2)(D)
        return {
            "u": _w((lambda *s: torch.randn(*s, dtype=torch.bfloat16) * 0.05)(B, L, D)),
            "A_re": _w(A_re),
            "A_im": _w(A_im),
            "B_re": _w(B_re),
            "B_im": _w(B_im),
            "C_re": _w(C_re),
            "C_im": _w(C_im),
            "log_dt": _w(log_dt),
            "Dskip": _w(Dskip),
        }
    _A_in = _A_setup()

    def _B_setup():
        B, S, D, F = 1, 2048, 512, 1376
        return {
            "x": _w((lambda *s: torch.randn(*s, dtype=torch.bfloat16) * 0.05)(B, S, D)),
            "w_gate": _w((lambda *s: torch.randn(*s, dtype=torch.bfloat16) * 0.05)(D, F)),
            "w_up": _w((lambda *s: torch.randn(*s, dtype=torch.bfloat16) * 0.05)(D, F)),
            "w_down": _w((lambda *s: torch.randn(*s, dtype=torch.bfloat16) * 0.05)(F, D)),
        }
    _B_in = _B_setup()

    return {
        "A_u": _A_in["u"],
        "A_A_re": _A_in["A_re"],
        "A_A_im": _A_in["A_im"],
        "A_B_re": _A_in["B_re"],
        "A_B_im": _A_in["B_im"],
        "A_C_re": _A_in["C_re"],
        "A_C_im": _A_in["C_im"],
        "A_log_dt": _A_in["log_dt"],
        "A_Dskip": _A_in["Dskip"],
        "B_x": _B_in["x"],
        "B_w_gate": _B_in["w_gate"],
        "B_w_up": _B_in["w_up"],
        "B_w_down": _B_in["w_down"],
    }

def reference(A_u, A_A_re, A_A_im, A_B_re, A_B_im, A_C_re, A_C_im, A_log_dt, A_Dskip, B_x, B_w_gate, B_w_up, B_w_down):
    def _A_ref(u, A_re, A_im, B_re, B_im, C_re, C_im, log_dt, Dskip):
        Bsz, L, D = u.shape
        dt = torch.exp(log_dt)[:, None]
        A = A_re + 1j * A_im
        Bc = B_re + 1j * B_im
        Cc = C_re + 1j * C_im
        logdA = dt * A
        dB = dt * Bc
        l = (lambda x, dt: x.to(dt))(torch.arange(L), A_re.dtype)
        P = torch.exp(logdA[:, :, None] * l[None, None, :])
        K = torch.einsum("dn,dnl,dn->dl", Cc, P, dB).real
        L2 = 2 * L
        Kp = _pad(K, ((0, 0), (0, L)))
        up = (lambda x, *d: x.permute(*d))(u, 0, 2, 1)
        up = _pad(up, ((0, 0), (0, 0), (0, L)))
        Kf = (lambda x, axes=(-2,-1): torch.fft.rfft2(x.float(), dim=axes))(Kp[:, None, :], axes=(-2, -1))
        Uf = (lambda x, axes=(-2,-1): torch.fft.rfft2(x.float(), dim=axes))(up[:, :, None, :], axes=(-2, -1))
        Yf = Uf * Kf[None, :, :, :]
        y = (lambda x, s, axes=(-2,-1): torch.fft.irfft2(x, s=s, dim=axes).to(torch.bfloat16))(Yf, (1, L2), axes=(-2, -1))[:, :, 0, :L]
        y = (lambda x, *d: x.permute(*d))(y, 0, 2, 1) + u * Dskip[None, None, :]
        return y

    def _B_ref(x, w_gate, w_up, w_down):
        g = _silu(x @ w_gate)
        u = x @ w_up
        return (g * u) @ w_down

    x = A_u
    xn = _layernorm(x, axis=-1)
    return x + _A_ref(xn, A_A_re, A_A_im, A_B_re, A_B_im, A_C_re, A_C_im, A_log_dt, A_Dskip) + _B_ref(xn, B_w_gate, B_w_up, B_w_down)
```

```cpp
#include <hip/hip_runtime.h>
#include <hip/hip_bf16.h>
#define ROUND_L 0
typedef unsigned short u16;
constexpr int L = 2048, D = 512, N = 128, F = 1376;
__device__ __forceinline__ float bf2f(u16 b) { return __builtin_bit_cast(float, (unsigned)b << 16); }
__device__ __forceinline__ u16 f2bf(float f) { unsigned u = __builtin_bit_cast(unsigned, f); u += 0x7FFFu + ((u >> 16) & 1u); return (u16)(u >> 16); }
__device__ __forceinline__ float rbf(float f) { return bf2f(f2bf(f)); }

__global__ void k_ln(const u16* x, u16* xn) {
  int t = blockIdx.x; int lane = threadIdx.x;
  float v[8]; float s = 0;
  for (int i = 0; i < 8; ++i) { v[i] = bf2f(x[t * D + lane * 8 + i]); s += v[i]; }
  for (int o = 32; o; o >>= 1) s += __shfl_xor(s, o);
  float mean = s / D; float q = 0;
  for (int i = 0; i < 8; ++i) { float d = v[i] - mean; q += d * d; }
  for (int o = 32; o; o >>= 1) q += __shfl_xor(q, o);
  float m = rbf(mean); float var = rbf(q / D);
  float sd = rbf(sqrtf(rbf(var + 1e-5f)));
  for (int i = 0; i < 8; ++i) xn[t * D + lane * 8 + i] = f2bf(rbf(v[i] - m) / sd);
}
__global__ void k_kgen(const u16* Are, const u16* Aim, const u16* Bre, const u16* Bim, const u16* Cre, const u16* Cim, const u16* logdt, float* K) {
  int d = blockIdx.y; int l = blockIdx.x * 256 + threadIdx.x;
  float dt = rbf(expf(bf2f(logdt[d])));
  float lf = ROUND_L ? rbf((float)l) : (float)l;
  float acc = 0;
  for (int n = 0; n < N; ++n) {
    float ar = dt * bf2f(Are[d * N + n]), ai = dt * bf2f(Aim[d * N + n]);
    float br = dt * bf2f(Bre[d * N + n]), bi = dt * bf2f(Bim[d * N + n]);
    float cr = bf2f(Cre[d * N + n]), ci = bf2f(Cim[d * N + n]);
    float pr = cr * br - ci * bi, pi = cr * bi + ci * br;
    float e = expf(ar * lf); float sn, cs; sincosf(ai * lf, &sn, &cs);
    acc += e * (pr * cs - pi * sn);
  }
  K[d * L + l] = acc;
}
__global__ void k_conv(const float* K, const u16* xn, const u16* Dsk, u16* A) {
  int d = threadIdx.x + blockIdx.x * 64; int t = blockIdx.y;
  float acc = 0;
  for (int s = 0; s <= t; ++s) acc += K[d * L + s] * bf2f(xn[(t - s) * D + d]);
  float y = rbf(acc);
  A[t * D + d] = f2bf(y + rbf(bf2f(xn[t * D + d]) * bf2f(Dsk[d])));
}
__global__ void k_ffn1(const u16* xn, const u16* wg, const u16* wu, u16* h) {
  int f = threadIdx.x + blockIdx.x * 32; int t = blockIdx.y * 8 + threadIdx.y;
  float g = 0, u = 0;
  for (int k = 0; k < D; ++k) { float a = bf2f(xn[t * D + k]); g += a * bf2f(wg[k * F + f]); u += a * bf2f(wu[k * F + f]); }
  g = rbf(g); u = rbf(u);
  float sg = rbf(g / (1.f + expf(-g)));
  h[t * F + f] = f2bf(sg * u);
}
__global__ void k_ffn2(const u16* h, const u16* wd, const u16* x, const u16* A, u16* out) {
  int d = threadIdx.x + blockIdx.x * 32; int t = blockIdx.y * 8 + threadIdx.y;
  float o = 0;
  for (int k = 0; k < F; ++k) o += bf2f(h[t * F + k]) * bf2f(wd[k * D + d]);
  o = rbf(o);
  out[t * D + d] = f2bf(rbf(bf2f(x[t * D + d]) + bf2f(A[t * D + d])) + o);
}
extern "C" void kernel_launch(void* const* d_in, const int* in_sizes, int n_in, void* d_out, int out_size, void* d_ws, size_t ws_size, hipStream_t stream) {
  const u16* x = (const u16*)d_in[0];
  char* ws = (char*)d_ws;
  u16* xn = (u16*)ws; float* K = (float*)(ws + (4 << 20)); u16* A = (u16*)(ws + (12 << 20)); u16* h = (u16*)(ws + (16 << 20));
  k_ln<<<L, 64, 0, stream>>>(x, xn);
  k_kgen<<<dim3(L / 256, D), 256, 0, stream>>>((const u16*)d_in[1], (const u16*)d_in[2], (const u16*)d_in[3], (const u16*)d_in[4], (const u16*)d_in[5], (const u16*)d_in[6], (const u16*)d_in[7], K);
  k_conv<<<dim3(D / 64, L), 64, 0, stream>>>(K, xn, (const u16*)d_in[8], A);
  k_ffn1<<<dim3(F / 32, L / 8), dim3(32, 8), 0, stream>>>(xn, (const u16*)d_in[10], (const u16*)d_in[11], h);
  k_ffn2<<<dim3(D / 32, L / 8), dim3(32, 8), 0, stream>>>(h, (const u16*)d_in[12], x, A, (u16*)d_out);
}
```

```cpp
#include <hip/hip_runtime.h>
#include <hip/hip_bf16.h>

typedef unsigned short u16;
typedef unsigned v4u __attribute__((ext_vector_type(4)));
typedef unsigned v2u __attribute__((ext_vector_type(2)));
typedef short s16x4 __attribute__((ext_vector_type(4)));
typedef __bf16 bf16x8 __attribute__((ext_vector_type(8)));
typedef __bf16 bf16x2 __attribute__((ext_vector_type(2)));
typedef float f32x2 __attribute__((ext_vector_type(2)));
typedef float f32x4 __attribute__((ext_vector_type(4)));
typedef float f32x16 __attribute__((ext_vector_type(16)));
#define LAS __attribute__((address_space(3)))
#define GAS __attribute__((address_space(1)))
#define DEVI __device__ __forceinline__

#define STAMPS 0

constexpr int SEQ = 2048, DM = 512, NMODE = 128, FF = 1376;
constexpr int WS_EPOCH = 0;
constexpr int WS_F1 = 4096;
constexpr int WS_F2 = 8192;
constexpr int WS_F3 = 12288;
constexpr int WS_STAT = 16384;
constexpr int WS_STAMP = 65536;
constexpr int WS_XT = 1 << 20;
constexpr int WS_YT = 3 << 20;
constexpr int WS_H = 5 << 20;
constexpr int H_BYTES = 64 * 86 * 1024;
constexpr int WS_END = WS_H + H_BYTES;
constexpr int RS = 41024, WGO = 16384, WUO = 16384 + 12320;
constexpr int CS = 4160;
constexpr int L_KIMG = 3 * RS;
constexpr int L_UIMG = L_KIMG + 4 * CS;
constexpr int US = 4608;
constexpr int L_PRM = L_UIMG + 2 * US;
constexpr int L_KTAB = L_PRM + 4096;
constexpr int KTS = 648 * 4;
constexpr int L_STAT = L_KTAB + 2 * KTS;
constexpr int L_CSUM = L_STAT + 512;
constexpr int L_K2048 = L_CSUM + 1536;
constexpr int L_LNST = L_K2048 + 16;
constexpr int L_CNT = L_LNST + 64;
constexpr int L_TOTAL = L_CNT + 64;
static_assert(L_TOTAL <= 163840, "lds");
constexpr int LNS = 1088;
constexpr int WDS = 30720;
constexpr int L_BASE = L_KIMG;
constexpr int L_YT = L_KIMG + 8192;
constexpr unsigned SPIN_CAP = 1u << 17;

DEVI float bf2f(unsigned b) { return __builtin_bit_cast(float, b << 16); }
DEVI float bflo(unsigned w) { return __builtin_bit_cast(float, w << 16); }
DEVI float bfhi(unsigned w) { return __builtin_bit_cast(float, w & 0xffff0000u); }
DEVI unsigned pk2(float a, float b) { f32x2 v = {a, b}; return __builtin_bit_cast(unsigned, __builtin_convertvector(v, bf16x2)); }
DEVI float rbf(float a) { return bflo(pk2(a, a)); }
DEVI f32x16 mfma32(v4u a, v4u b, f32x16 c) { return __builtin_amdgcn_mfma_f32_32x32x16_bf16(__builtin_bit_cast(bf16x8, a), __builtin_bit_cast(bf16x8, b), c, 0, 0, 0); }
DEVI f32x4 mfma16(v4u a, v4u b, f32x4 c) { return __builtin_amdgcn_mfma_f32_16x16x32_bf16(__builtin_bit_cast(bf16x8, a), __builtin_bit_cast(bf16x8, b), c, 0, 0, 0); }
DEVI v2u trd(LAS char* p) { return __builtin_bit_cast(v2u, __builtin_amdgcn_ds_read_tr16_b64_v4i16((LAS s16x4*)p)); }
DEVI v4u trd2(LAS char* p0, LAS char* p1) { v2u a = trd(p0), b = trd(p1); return v4u{a.x, a.y, b.x, b.y}; }
DEVI void dma16(const void* sbase, unsigned voff, unsigned ldsaddr) {
  asm volatile("s_mov_b32 m0, %2\n\ts_nop 0\n\tglobal_load_lds_dwordx4 %0, %1" :: "v"(voff), "s"(sbase), "s"(ldsaddr) : "memory");
}
template <int N> DEVI void waitv() { asm volatile("s_waitcnt vmcnt(%0)" :: "i"(N) : "memory"); }
DEVI void bar() { asm volatile("s_waitcnt lgkmcnt(0)\n\ts_barrier" ::: "memory"); }
DEVI void sched0() { __builtin_amdgcn_sched_barrier(0); }
DEVI void ld16(v4u& r, const void* sbase, unsigned voff) { asm volatile("global_load_dwordx4 %0, %1, %2" : "=v"(r) : "v"(voff), "s"(sbase) : "memory"); }
DEVI void ld16c(v4u& r, const void* sbase, unsigned voff) { asm volatile("global_load_dwordx4 %0, %1, %2 sc1" : "=v"(r) : "v"(voff), "s"(sbase) : "memory"); }
DEVI void ld4c(unsigned& r, const void* sbase, unsigned voff) { asm volatile("global_load_dword %0, %1, %2 sc1" : "=v"(r) : "v"(voff), "s"(sbase) : "memory"); }
DEVI void ld2(unsigned& r, const void* sbase, unsigned voff) { asm volatile("global_load_ushort %0, %1, %2" : "=v"(r) : "v"(voff), "s"(sbase) : "memory"); }
DEVI void st16c(void* sbase, unsigned voff, v4u v) { asm volatile("global_store_dwordx4 %0, %1, %2 sc1\n\ts_nop 1" :: "v"(voff), "v"(v), "s"(sbase) : "memory"); }
DEVI void st8c(void* sbase, unsigned voff, v2u v) { asm volatile("global_store_dwordx2 %0, %1, %2 sc1\n\ts_nop 1" :: "v"(voff), "v"(v), "s"(sbase) : "memory"); }
DEVI void st4c(void* sbase, unsigned voff, unsigned v) { asm volatile("global_store_dword %0, %1, %2 sc1\n\ts_nop 1" :: "v"(voff), "v"(v), "s"(sbase) : "memory"); }
#define PIN(x) asm volatile("" : "+v"(x))
DEVI float wsum(float v) {
#pragma unroll
  for (int o = 32; o; o >>= 1) v += __shfl_xor(v, o);
  return v;
}
DEVI unsigned lds_add(LAS char* L, int off) { return __hip_atomic_fetch_add((LAS unsigned*)(L + off), 1u, __ATOMIC_RELAXED, __HIP_MEMORY_SCOPE_WORKGROUP); }
DEVI void lds_arrive(LAS char* L, int off, int lane) { asm volatile("s_waitcnt lgkmcnt(0)" ::: "memory"); if (lane == 0) lds_add(L, off); }
DEVI void lds_wait(LAS char* L, int off, unsigned n) {
#pragma unroll 1
  for (unsigned sp = 0; sp < (1u << 24); ++sp) {
    unsigned v = __hip_atomic_load((LAS unsigned*)(L + off), __ATOMIC_RELAXED, __HIP_MEMORY_SCOPE_WORKGROUP);
    if (__builtin_amdgcn_readfirstlane(v) >= n) break;
    __builtin_amdgcn_s_sleep(1);
  }
  asm volatile("" ::: "memory");
}
#define STAMP(i)

struct Args {
  const u16 *x, *are, *aim, *bre, *bim, *cre, *cim, *logdt, *dsk, *wg, *wu, *wd;
  u16* out;
  char* ws;
};

__global__ void __launch_bounds__(512, 2) fused_kernel(Args a) {
  extern __shared__ __attribute__((aligned(16))) char smem[];
  LAS char* L = (LAS char*)smem;
  const unsigned lbase = (unsigned)(size_t)smem;
  STAMP(0);
  const int tid = threadIdx.x, lane = tid & 63;
  const int w = __builtin_amdgcn_readfirstlane(tid >> 6);
  const bool helper = w >= 4;
  const int hw = w & 3;
  const int b = blockIdx.x, xcd = b & 7, slot = b >> 3;
  const int m = (xcd >> 2) * 8 + (slot >> 2), j = (xcd & 3) * 4 + (slot & 3);
  const int rot1 = ((slot >> 2) + 2 * (slot & 3)) & 7;
  const int f0 = min(96 * j, FF - 96);
  const int m2 = xcd * 4 + (slot >> 3), n2 = slot & 7;
  const int rot2 = ((n2 * 11) / 4 + ((slot >> 3) * 11) / 2) % 22;
  char* ws = a.ws;

  unsigned codetouch = 0;
  {
    unsigned long long pc; unsigned sz;
    asm volatile("s_getpc_b64 %0\n.Lfk_start:\n\ts_mov_b32 %1, .Lfk_end-.Lfk_start" : "=s"(pc), "=s"(sz));
    unsigned off = (unsigned)((lane * 8 + (slot & 7)) * 128);
    if (w == 3 && off < sz) asm volatile("global_load_dword %0, %1, %2" : "=v"(codetouch) : "v"(off), "s"(pc) : "memory");
  }
  const int t0 = m * 128 + (j >> 1) * 16;
  v4u ra, rb;
  ld16(ra, a.x + (size_t)(t0 + w) * DM, lane * 16);
  ld16(rb, a.x + (size_t)(t0 + w + 8) * DM, lane * 16);
  unsigned p_are, p_aim, p_bre, p_bim, p_cre, p_cim, p_ldt;
  if (!helper) {
    const unsigned po = (unsigned)(b * 256 + tid) * 2;
    ld2(p_are, a.are, po); ld2(p_aim, a.aim, po); ld2(p_bre, a.bre, po); ld2(p_bim, a.bim, po);
    ld2(p_cre, a.cre, po); ld2(p_cim, a.cim, po); ld2(p_ldt, a.logdt, (unsigned)(2 * b + (tid >> 7)) * 2);
  }
  unsigned epoch_v;
  ld4c(epoch_v, ws + WS_EPOCH, 0u);
  if (tid < 16) *(LAS unsigned*)(L + L_CNT + tid * 4) = 0u;
  asm volatile("s_barrier" ::: "memory");

  unsigned vx0, vx1, vw0, vw1, vw2;
  {
    const int r = lane >> 3, cp = lane & 7;
    vx0 = r * 1024 + ((cp ^ (lane >> 4)) * 16);
    vx1 = r * 1024 + ((cp ^ (4 + (lane >> 4))) * 16);
    int n0 = lane, n1 = 64 + lane, n2_ = 128 + lane;
    vw0 = (n0 / 12) * (FF * 2) + (n0 % 12) * 16;
    vw1 = (n1 / 12) * (FF * 2) + (n1 % 12) * 16;
    vw2 = (n2_ / 12) * (FF * 2) + (n2_ % 12) * 16;
  }
  auto issue_stage = [&](int s, int sl) {
    const int ks = (s + rot1) & 7;
    const char* xs = (const char*)a.x + (size_t)(m * 128 + hw * 32) * 1024 + ks * 128;
    const unsigned lb = lbase + sl * RS;
    dma16(xs, vx0, lb + hw * 4096);
    dma16(xs + 8192, vx1, lb + hw * 4096 + 1024);
    dma16(xs + 16384, vx0, lb + hw * 4096 + 2048);
    dma16(xs + 24576, vx1, lb + hw * 4096 + 3072);
    const size_t wo = (size_t)(ks * 64 + hw * 16) * (FF * 2) + f0 * 2;
    const char* gs = (const char*)a.wg + wo;
    const char* us = (const char*)a.wu + wo;
    dma16(gs, vw0, lb + WGO + hw * 3072);
    dma16(gs, vw1, lb + WGO + hw * 3072 + 1024);
    dma16(gs, vw2, lb + WGO + hw * 3072 + 2048);
    dma16(us, vw0, lb + WUO + hw * 3072);
    dma16(us, vw1, lb + WUO + hw * 3072 + 1024);
    dma16(us, vw2, lb + WUO + hw * 3072 + 2048);
  };
  if (helper) { issue_stage(0, 0); issue_stage(1, 1); waitv<20>(); }
  else waitv<0>();
  PIN(ra); PIN(rb); PIN(codetouch); PIN(epoch_v);
  const unsigned epoch = __builtin_amdgcn_readfirstlane(epoch_v);
  const unsigned tag = epoch * 0x9E3779B1u + 0x7F4A7C15u;
  STAMP(1);

  {
    float va[8], vb[8];
#pragma unroll
    for (int i = 0; i < 4; ++i) { va[2 * i] = bflo(ra[i]); va[2 * i + 1] = bfhi(ra[i]); vb[2 * i] = bflo(rb[i]); vb[2 * i + 1] = bfhi(rb[i]); }
    float sa = 0.f, sb = 0.f;
#pragma unroll
    for (int i = 0; i < 8; ++i) { sa += va[i]; sb += vb[i]; }
    sa = wsum(sa) * (1.f / DM); sb = wsum(sb) * (1.f / DM);
    float qa = 0.f, qb = 0.f;
#pragma unroll
    for (int i = 0; i < 8; ++i) { float da = va[i] - sa, db = vb[i] - sb; qa += da * da; qb += db * db; }
    qa = wsum(qa) * (1.f / DM); qb = wsum(qb) * (1.f / DM);
    const float ma = rbf(sa), mb = rbf(sb);
    const float da = rbf(__builtin_sqrtf(rbf(rbf(qa) + 1e-5f))), db = rbf(__builtin_sqrtf(rbf(rbf(qb) + 1e-5f)));
    const float ia = 1.f / da, ib = 1.f / db;
    unsigned oa[4], ob[4];
#pragma unroll
    for (int i = 0; i < 4; ++i) {
      unsigned ca = pk2(va[2 * i] - ma, va[2 * i + 1] - ma), cb = pk2(vb[2 * i] - mb, vb[2 * i + 1] - mb);
      oa[i] = pk2(bflo(ca) * ia, bfhi(ca) * ia); ob[i] = pk2(bflo(cb) * ib, bfhi(cb) * ib);
    }
    if ((lane >> 5) == (j & 1)) {
      LAS char* p = L + L_UIMG + w * LNS + (lane & 31) * 32;
      *(LAS v4u*)p = v4u{oa[0], ob[0], oa[1], ob[1]};
      *(LAS v4u*)(p + 16) = v4u{oa[2], ob[2], oa[3], ob[3]};
    }
    if (lane == 0) {
      *(LAS unsigned*)(L + L_LNST + w * 4) = pk2(ma, da);
      *(LAS unsigned*)(L + L_LNST + 32 + w * 4) = pk2(mb, db);
    }
  }
  if (!helper) {
    PIN(p_are); PIN(p_aim); PIN(p_bre); PIN(p_bim); PIN(p_cre); PIN(p_cim); PIN(p_ldt);
    const float dt = rbf(expf(bf2f(p_ldt)));
    const float ar = (dt * bf2f(p_are)) * 1.44269504088896341f;
    const float ai = (dt * bf2f(p_aim)) * 0.15915494309189535f;
    const float br = dt * bf2f(p_bre), bi = dt * bf2f(p_bim), cr0 = bf2f(p_cre), ci0 = bf2f(p_cim);
    const float cr = cr0 * br - ci0 * bi, ci = cr0 * bi + ci0 * br;
    *(LAS f32x4*)(L + L_PRM + tid * 16) = f32x4{ar, ai, cr, ci};
    const float mag = __builtin_amdgcn_exp2f(ar * 2048.f), th = __builtin_amdgcn_fractf(ai * 2048.f);
    const float kv = wsum(mag * (cr * __builtin_amdgcn_cosf(th) - ci * __builtin_amdgcn_sinf(th)));
    if (lane == 0) *(LAS float*)(L + L_K2048 + w * 4) = kv;
  }
  bar();
  STAMP(2);
  if (!helper) {
#pragma unroll
    for (int it = 0; it < 2; ++it) {
      const int p0 = (w * 2 + it) * 16 + (lane >> 4) * 4;
      LAS char* p = L + L_UIMG + ((lane & 15) >> 2) * LNS + (p0 + (lane & 3)) * 8;
      const v4u v = trd2(p, p + 4 * LNS);
      const int pair = (j & 1) * 128 + p0 + ((lane & 15) >> 2);
      st16c(ws + WS_XT, (unsigned)(pair * 8192 + (t0 >> 4) * 64 + (lane & 3) * 16), v);
    }
    if (w == 0 && !(j & 1) && lane < 16) {
      const unsigned sv = *(LAS unsigned*)(L + L_LNST + lane * 4);
      st8c(ws + WS_STAT, (unsigned)(t0 + lane) * 8, v2u{sv, tag});
    }
  }
  {
    const int cl = w >> 2, g = lane >> 4;
    const float fi = (float)(lane & 15);
    f32x4 d1 = {}, d2 = {}, d4 = {}, d8 = {};
#pragma unroll 1
    for (int kk = 0; kk < 2; ++kk) {
      v4u fa[4], fb[4];
#pragma unroll
      for (int e = 0; e < 4; ++e) {
        const f32x4 p = *(LAS f32x4*)(L + L_PRM + (cl * 128 + (hw * 2 + kk) * 16 + g * 4 + e) * 16);
        const float mag = __builtin_amdgcn_exp2f(p.x * fi), th = __builtin_amdgcn_fractf(p.y * fi);
        float zr = mag * __builtin_amdgcn_cosf(th), zi = mag * __builtin_amdgcn_sinf(th);
#pragma unroll
        for (int s = 0; s < 4; ++s) {
          fa[s][e] = pk2(zr, zi);
          const float t = zr * zi; zr = zr * zr - zi * zi; zi = t + t;
        }
#pragma unroll
        for (int s = 0; s < 4; ++s) {
          fb[s][e] = pk2(p.z * zr - p.w * zi, -(p.z * zi) - p.w * zr);
          if (s < 3) { const float t = zr * zi; zr = zr * zr - zi * zi; zi = t + t; }
        }
      }
      d1 = mfma16(fa[0], fb[0], d1); d2 = mfma16(fa[1], fb[1], d2); d4 = mfma16(fa[2], fb[2], d4); d8 = mfma16(fa[3], fb[3], d8);
    }
    LAS char* pp = L + 2 * RS + w * 2560 + ((lane & 15) * 16 + g * 4) * 4;
    *(LAS f32x4*)pp = d1;
    if (lane & 8) { *(LAS f32x4*)(pp + 512) = d2; *(LAS f32x4*)(pp + 1024) = d4; *(LAS f32x4*)(pp + 1536) = d8; }
  }
  bar();
  if (tid < 320) {
    const int cl = tid >= 160, q = tid - cl * 160;
    LAS char* pp = L + 2 * RS + cl * 10240 + q * 16;
    const f32x4 s = (*(LAS f32x4*)pp + *(LAS f32x4*)(pp + 2560)) + (*(LAS f32x4*)(pp + 5120) + *(LAS f32x4*)(pp + 7680));
    *(LAS f32x4*)(L + L_KTAB + cl * KTS + q * 16) = s;
  } else if (tid < 322) {
    const int cl = tid - 320;
    *(LAS float*)(L + L_KTAB + cl * KTS + 2560) = *(LAS float*)(L + L_K2048 + cl * 8) + *(LAS float*)(L + L_K2048 + cl * 8 + 4);
  }
  STAMP(3);

  if (!helper) {
    const int tg = w & 1, fh = w >> 1;
    const int r = lane & 31, h = lane >> 5;
    f32x16 acc[2][3];
#pragma unroll
    for (int i = 0; i < 2; ++i)
#pragma unroll
      for (int c = 0; c < 3; ++c) acc[i][c] = f32x16{};
    f32x4 cs[3] = {};
    const unsigned one2 = ((lane & 15) == ((lane >> 4) & 1)) ? 0x3F803F80u : 0u;
    const v4u sel = {one2, one2, one2, one2};
    int xo[4];
#pragma unroll
    for (int ks = 0; ks < 4; ++ks) xo[ks] = (tg * 64 + r) * 128 + (((2 * ks + h) ^ ((r >> 1) & 7)) * 16);
    const int wo = WGO + ((lane >> 4) & 1) * 12320 + (8 * h + ((lane & 15) >> 2)) * 192 + fh * 96 + (lane & 3) * 8;
    v4u xf[2][2], wf[2][3];
    auto ldf = [&](int buf, LAS char* S, int ks) {
      xf[buf][0] = *(LAS v4u*)(S + xo[ks]);
      xf[buf][1] = *(LAS v4u*)(S + xo[ks] + 4096);
#pragma unroll
      for (int c = 0; c < 3; ++c) wf[buf][c] = trd2(S + wo + ks * 3072 + c * 32, S + wo + ks * 3072 + 768 + c * 32);
    };
    auto mm = [&](int buf) {
#pragma unroll
      for (int c = 0; c < 3; ++c) { acc[0][c] = mfma32(wf[buf][c], xf[buf][0], acc[0][c]); acc[1][c] = mfma32(wf[buf][c], xf[buf][1], acc[1][c]); }
    };
    auto msel = [&](int buf) {
#pragma unroll
      for (int c = 0; c < 3; ++c) cs[c] = mfma16(sel, wf[buf][c], cs[c]);
    };
    bar();
    STAMP(4);
    int sl = 0;
    ldf(0, L, 0);
#pragma unroll 1
    for (int s = 0; s < 8; ++s) {
      LAS char* S = L + sl * RS;
      ldf(1, S, 1); sched0();
      mm(0); if (tg == 0) msel(0); sched0();
      ldf(0, S, 2); sched0();
      mm(1); if (tg == 1) msel(1); sched0();
      ldf(1, S, 3); sched0();
      mm(0); if (tg == 0) msel(0); sched0();
      if (tg == 1) msel(1);
      if (s == 7 && lane < 16) {
#pragma unroll
        for (int c = 0; c < 3; ++c) {
          *(LAS float*)(L + L_CSUM + tg * 768 + ((fh * 3 + c) * 32 + lane) * 4) = cs[c][0];
          *(LAS float*)(L + L_CSUM + tg * 768 + ((fh * 3 + c) * 32 + 16 + lane) * 4) = cs[c][1];
        }
      }
      if (s == 0) waitv<0>();
      sched0();
      bar();
      if (s == 0 && tid == 0) st4c(ws + WS_F1, (unsigned)b * 4, tag);
      sl = sl == 2 ? 0 : sl + 1;
      if (s < 7) ldf(0, L + sl * RS, 0);
      sched0();
      mm(1); sched0();
    }
    STAMP(5);
    float c1[2], ir2[2];
    {
      v4u xa[2];
#pragma unroll
      for (int i = 0; i < 2; ++i) {
        const unsigned sv = *(LAS unsigned*)(L + L_STAT + (tg * 64 + i * 32 + r) * 4);
        const unsigned nm = (sv & 0xffffu) ^ 0x8000u;
        xa[i] = h ? v4u{0u, 0u, 0u, 0u} : v4u{nm | (nm << 16), nm, 0u, 0u};
        const float sd = bfhi(sv);
        c1[i] = -1.44269504088896341f / sd; ir2[i] = sd * sd;
      }
#pragma unroll
      for (int c = 0; c < 3; ++c) {
        const int o = L_CSUM + ((fh * 3 + c) * 32 + r) * 4;
        const float s = *(LAS float*)(L + o) + *(LAS float*)(L + o + 768);
        const float hi = rbf(s), mid = rbf(s - hi), lo = (s - hi) - mid;
        const v4u wa = h ? v4u{0u, 0u, 0u, 0u} : v4u{pk2(hi, mid), pk2(lo, 0.f), 0u, 0u};
        acc[0][c] = mfma32(wa, xa[0], acc[0][c]); acc[1][c] = mfma32(wa, xa[1], acc[1][c]);
      }
    }
#pragma unroll
    for (int c = 0; c < 3; ++c) {
      const int F16 = f0 / 16 + fh * 3 + c;
#pragma unroll
      for (int i = 0; i < 2; ++i) {
        float hv[8];
#pragma unroll
        for (int e = 0; e < 8; ++e) {
          const float g = acc[i][c][e], u = acc[i][c][e + 8];
          const float ex = __builtin_amdgcn_exp2f(g * c1[i]);
          hv[e] = (g * u) * __builtin_amdgcn_rcpf(ex * ir2[i] + ir2[i]);
        }
        const v4u o = {pk2(hv[0], hv[1]), pk2(hv[2], hv[3]), pk2(hv[4], hv[5]), pk2(hv[6], hv[7])};
        if (F16 >= 6 * j) st16c(ws + WS_H + (size_t)((m * 4 + tg * 2 + i) * 86 + F16) * 1024, lane * 16, o);
      }
    }
    STAMP(6);
    waitv<0>();
    if (lane == 0) { if (lds_add(L, L_CNT + 0) == 3u && j < 15) st4c(ws + WS_F2, (unsigned)(m * 128 + j * 4), tag); }
    STAMP(7);
    const int kq = w;
    LAS char* W = L + kq * WDS;
    const int q = (lane & 15) >> 2;
    const int wa0 = (4 * h + q) * 128 + (q >> 1) * 64 + ((lane >> 4) & 1) * 32 + (lane & 3) * 8;
    const int wa1 = wa0 ^ 64;
    const unsigned vd = (lane >> 3) * 1024 + (((lane & 7) ^ (((lane >> 4) & 1) << 2)) * 16);
    lds_wait(L, L_CNT + 16 + kq * 4, 1u);
    v4u we[7][2];
#pragma unroll
    for (int i = 0; i < 7; ++i) { we[i][0] = trd2(W + i * 2048 + wa0, W + i * 2048 + 1024 + wa0); we[i][1] = trd2(W + i * 2048 + wa1, W + i * 2048 + 1024 + wa1); }
#pragma unroll
    for (int i = 0; i < 7; ++i) { PIN(we[i][0]); PIN(we[i][1]); }
    asm volatile("s_waitcnt lgkmcnt(0)" ::: "memory");
#pragma unroll
    for (int i = 15; i < 22; ++i) {
      int ii = i + rot2; ii = ii >= 22 ? ii - 22 : ii;
      const int F16 = min(22 * kq + ii, 85);
      const char* src = (const char*)a.wd + (size_t)F16 * 16384 + n2 * 128;
      dma16(src, vd, lbase + kq * WDS + (i - 15) * 2048);
      dma16(src + 8192, vd, lbase + kq * WDS + (i - 15) * 2048 + 1024);
    }
    STAMP(8);
    {
      const int jlo = (22 * kq) / 6, jhi = min(22 * kq + 21, 85) / 6;
      const bool need = lane >= jlo && lane <= jhi;
      _Pragma("unroll 1") for (unsigned sp = 0; sp < SPIN_CAP; ++sp) {
        unsigned fv; ld4c(fv, ws + WS_F2 + (m2 >> 1) * 128, (unsigned)(lane & 15) * 4);
        waitv<0>(); PIN(fv);
        if (__all(!need || fv == tag)) break;
        __builtin_amdgcn_s_sleep(2);
      }
    }
    STAMP(9);
    const __amdgpu_buffer_rsrc_t rh = __builtin_amdgcn_make_buffer_rsrc((void*)(ws + WS_H), 0, H_BYTES, 0x00020000);
    constexpr int HW = 8;
    v4u hf[HW][2];
    auto ldh = [&](int i) {
      int ii = i + rot2; ii = ii >= 22 ? ii - 22 : ii;
      const int F16 = 22 * kq + ii;
      const int so = F16 < 86 ? (m2 * 2 * 86 + F16) * 1024 : 0x40000000;
      hf[i % HW][0] = __builtin_amdgcn_raw_buffer_load_b128(rh, lane * 16, so, 16);
      hf[i % HW][1] = __builtin_amdgcn_raw_buffer_load_b128(rh, lane * 16, so + 86 * 1024, 16);
    };
#pragma unroll
    for (int i = 0; i < HW; ++i) ldh(i);
    sched0();
    f32x16 o2[2][2];
#pragma unroll
    for (int i = 0; i < 4; ++i) o2[i >> 1][i & 1] = f32x16{};
#pragma unroll
    for (int i = 0; i < 22; ++i) {
      v4u w0, w1;
      if (i < 7) { w0 = we[i][0]; w1 = we[i][1]; }
      else { const int pos = (i < 15 ? i : i - 15) * 2048; w0 = trd2(W + pos + wa0, W + pos + 1024 + wa0); w1 = trd2(W + pos + wa1, W + pos + 1024 + wa1); }
      const v4u h0 = hf[i % HW][0], h1 = hf[i % HW][1];
      o2[0][0] = mfma32(w0, h0, o2[0][0]); o2[1][0] = mfma32(w1, h0, o2[1][0]);
      o2[0][1] = mfma32(w0, h1, o2[0][1]); o2[1][1] = mfma32(w1, h1, o2[1][1]);
      if (i + HW < 22) ldh(i + HW);
      sched0();
    }
    STAMP(10);
#pragma unroll
    for (int cc = 0; cc < 2; ++cc)
#pragma unroll
      for (int tt = 0; tt < 2; ++tt)
#pragma unroll
        for (int g4 = 0; g4 < 4; ++g4) {
          const int tok = tt * 32 + r, ch4 = cc * 8 + g4 * 2 + h;
          *(LAS f32x4*)(W + tok * 256 + ((ch4 ^ (tok & 15)) * 16)) = f32x4{o2[cc][tt][4 * g4], o2[cc][tt][4 * g4 + 1], o2[cc][tt][4 * g4 + 2], o2[cc][tt][4 * g4 + 3]};
        }
  } else {
    const int ht = tid - 256;
    v4u pst, pfl;
    int sl = 0;
#pragma unroll 1
    for (int s = 0; s < 6; ++s) {
      waitv<10>();
      bar();
      issue_stage(s + 2, sl == 0 ? 2 : sl - 1);
      sl = sl == 2 ? 0 : sl + 1;
      if (s == 0) {
#pragma unroll 1
        for (int id = ht; id < 520; id += 256) {
          const int cl = id >= 260, m0 = (id - cl * 260) * 8;
          float v[9];
#pragma unroll
          for (int e = 0; e < 9; ++e) {
            const int lag = 2047 - m0 - e;
            int big = (int)(pk2((float)lag, 0.f) & 0xffffu) - 0x4300 + 128;
            PIN(big);
            const int idx = lag < 128 ? max(lag, 0) : big;
            const float kv = *(LAS float*)(L + L_KTAB + cl * KTS + idx * 4);
            v[e] = lag < 0 ? 0.f : kv;
          }
          *(LAS v4u*)(L + L_KIMG + cl * 2 * CS + m0 * 2) = v4u{pk2(v[0], v[1]), pk2(v[2], v[3]), pk2(v[4], v[5]), pk2(v[6], v[7])};
          *(LAS v4u*)(L + L_KIMG + cl * 2 * CS + CS + m0 * 2) = v4u{pk2(v[1], v[2]), pk2(v[3], v[4]), pk2(v[5], v[6]), pk2(v[7], v[8])};
        }
        if (ht < 64) *(LAS v4u*)(L + L_UIMG + (ht >> 5) * US + (ht & 31) * 16) = v4u{0u, 0u, 0u, 0u};
      }
    }
    ld16c(pst, ws + WS_STAT + m * 1024, lane * 16); ld16c(pfl, ws + WS_F1, lane * 16);
    waitv<12>(); bar();
    waitv<2>(); bar();
    STAMP(4);
    waitv<0>(); PIN(pst); PIN(pfl);
    _Pragma("unroll 1") for (unsigned sp = 0; sp < SPIN_CAP; ++sp) {
      if (__all(pst.y == tag && pst.w == tag)) break;
      __builtin_amdgcn_s_sleep(2);
      ld16c(pst, ws + WS_STAT + m * 1024, lane * 16); waitv<0>(); PIN(pst);
    }
    if (hw == 0) *(LAS v2u*)(L + L_STAT + lane * 8) = v2u{pst.x, pst.z};
    v4u u0, u1;
    const bool e1 = __all(pfl.x == tag && pfl.y == tag && pfl.z == tag && pfl.w == tag);
    if (e1) { ld16c(u0, ws + WS_XT + b * 8192, ht * 16); ld16c(u1, ws + WS_XT + b * 8192 + 4096, ht * 16); }
    bar();
    if (!e1) {
      _Pragma("unroll 1") for (unsigned sp = 0; sp < SPIN_CAP; ++sp) {
        __builtin_amdgcn_s_sleep(2);
        ld16c(pfl, ws + WS_F1, lane * 16); waitv<0>(); PIN(pfl);
        if (__all(pfl.x == tag && pfl.y == tag && pfl.z == tag && pfl.w == tag)) break;
      }
      ld16c(u0, ws + WS_XT + b * 8192, ht * 16); ld16c(u1, ws + WS_XT + b * 8192 + 4096, ht * 16);
    }
    STAMP(5);
    const unsigned vd = (lane >> 3) * 1024 + (((lane & 7) ^ (((lane >> 4) & 1) << 2)) * 16);
    auto issue_wd = [&](int i) {
      int ii = i + rot2; ii = ii >= 22 ? ii - 22 : ii;
      const int F16 = min(22 * hw + ii, 85);
      const char* src = (const char*)a.wd + (size_t)F16 * 16384 + n2 * 128;
      dma16(src, vd, lbase + hw * WDS + i * 2048);
      dma16(src + 8192, vd, lbase + hw * WDS + i * 2048 + 1024);
    };
    constexpr int WDE = 4;
#pragma unroll
    for (int i = 0; i < WDE; ++i) issue_wd(i);
    waitv<2 * WDE>(); PIN(u0); PIN(u1);
    if (b == 0 && ht == 0) st4c(ws + WS_EPOCH, 0u, epoch + 1u);
    {
      const int sub = ht & 3, g = ht >> 2;
      LAS char* p = L + L_UIMG + (sub & 1) * US + 512 + (g * 16 + (sub >> 1) * 8) * 2;
      *(LAS v4u*)p = u0;
      *(LAS v4u*)(p + 2048) = u1;
    }
    lds_arrive(L, L_CNT + 4, lane);
#pragma unroll
    for (int i = WDE; i < 15; ++i) issue_wd(i);
    lds_wait(L, L_CNT + 4, 4u);
    STAMP(6);
    const int cl = hw >> 1, rh_ = hw & 1, i16 = lane & 15, g = lane >> 4;
    f32x4 y[8];
#pragma unroll
    for (int t = 0; t < 8; ++t) y[t] = f32x4{};
    {
      const int s0 = 2031 - i16 + 8 * g;
      LAS char* ka = L + L_KIMG + cl * 2 * CS + (s0 & 1) * CS + (s0 >> 1) * 4;
      LAS char* ub = L + L_UIMG + cl * US + 512 + (16 * i16 - 16 + 8 * g) * 2;
#pragma unroll 1
      for (int rr = 0; rr < 4; ++rr) {
        const int r = rh_ * 4 + rr;
        v4u fa[8], fb[8];
#pragma unroll
        for (int q = 0; q < 8; ++q) {
          LAS unsigned* p = (LAS unsigned*)(ka - q * 512 - r * 64);
          fa[q] = v4u{p[0], p[1], p[2], p[3]};
          fb[q] = *(LAS v4u*)(ub + q * 512 - r * 64);
        }
#pragma unroll
        for (int q = 0; q < 8; ++q)
#pragma unroll
          for (int t = q; t < 8; ++t) y[t] = mfma16(fa[q], fb[t - q], y[t]);
        if (rr == 1) { waitv<0>(); if (lane == 0) lds_add(L, L_CNT + 16 + hw * 4); }
      }
    }
    STAMP(7);
    lds_arrive(L, L_CNT + 8, lane);
    lds_wait(L, L_CNT + 8, 4u);
    if (rh_) {
#pragma unroll
      for (int t = 0; t < 4; ++t) { const f32x4 tmp = y[t]; y[t] = y[t + 4]; y[t + 4] = tmp; PIN(y[t]); PIN(y[t + 4]); }
    }
#pragma unroll
    for (int t = 0; t < 4; ++t) *(LAS f32x4*)(L + L_KIMG + hw * 4096 + t * 1024 + lane * 16) = y[t + 4];
    lds_arrive(L, L_CNT + 12, lane);
    const float dk = bf2f(a.dsk[2 * b + cl]);
    lds_wait(L, L_CNT + 12, 4u);
#pragma unroll
    for (int t = 0; t < 4; ++t) {
      const f32x4 o = *(LAS f32x4*)(L + L_KIMG + (hw ^ 1) * 4096 + t * 1024 + lane * 16);
      const f32x4 s = y[t] + o;
      LAS v2u* up = (LAS v2u*)(L + L_UIMG + cl * US + 512 + ((rh_ * 4 + t) * 256 + i16 * 16 + g * 4) * 2);
      const v2u uv = *up;
      const unsigned y01 = pk2(s[0], s[1]), y23 = pk2(s[2], s[3]);
      const unsigned k01 = pk2(bflo(uv.x) * dk, bfhi(uv.x) * dk), k23 = pk2(bflo(uv.y) * dk, bfhi(uv.y) * dk);
      *up = v2u{pk2(bflo(y01) + bflo(k01), bfhi(y01) + bfhi(k01)), pk2(bflo(y23) + bflo(k23), bfhi(y23) + bfhi(k23))};
    }
    asm volatile("s_waitcnt lgkmcnt(0)" ::: "memory");
#pragma unroll
    for (int it = 0; it < 2; ++it) {
      const int t = rh_ * 1024 + it * 512 + lane * 8;
      const v4u v = *(LAS v4u*)(L + L_UIMG + cl * US + 512 + t * 2);
      const int ch = 2 * b + cl;
      st16c(ws + WS_YT, (unsigned)((((t >> 6) * 8 + (ch >> 6)) * 64 + (ch & 63)) * 128 + (t & 63) * 2), v);
    }
    waitv<0>();
    if (lane == 0) { if (lds_add(L, L_CNT + 32) == 3u) st4c(ws + WS_F3, (unsigned)b * 4, tag); }
    STAMP(8);
    v4u x0, x1;
    ld16(x0, a.x + (size_t)(m2 * 64) * DM + n2 * 64, (unsigned)((ht >> 3) * 1024 + (ht & 7) * 16));
    ld16(x1, a.x + (size_t)(m2 * 64 + 32) * DM + n2 * 64, (unsigned)((ht >> 3) * 1024 + (ht & 7) * 16));
    _Pragma("unroll 1") for (unsigned sp = 0; sp < SPIN_CAP; ++sp) {
      unsigned fv; ld4c(fv, ws + WS_F3 + n2 * 128, (unsigned)(lane & 31) * 4);
      waitv<0>(); PIN(fv);
      if (__all(fv == tag)) break;
      __builtin_amdgcn_s_sleep(8);
    }
    PIN(x0); PIN(x1);
    STAMP(9);
    v4u a0, a1;
    ld16c(a0, ws + WS_YT + (m2 * 8 + n2) * 8192, ht * 16); ld16c(a1, ws + WS_YT + (m2 * 8 + n2) * 8192 + 4096, ht * 16);
    lds_wait(L, L_CNT + 32, 4u);
    *(LAS v4u*)(L + L_BASE + (ht >> 3) * 128 + (((ht & 7) ^ ((ht >> 3) & 7)) * 16)) = x0;
    *(LAS v4u*)(L + L_BASE + 4096 + (ht >> 3) * 128 + (((ht & 7) ^ ((ht >> 3) & 7)) * 16)) = x1;
    waitv<0>(); PIN(a0); PIN(a1);
    *(LAS v4u*)(L + L_YT + ht * 16) = a0;
    *(LAS v4u*)(L + L_YT + 4096 + ht * 16) = a1;
    lds_arrive(L, L_CNT + 36, lane);
    lds_wait(L, L_CNT + 36, 4u);
#pragma unroll
    for (int it = 0; it < 2; ++it) {
      const int c8 = hw * 2 + it;
      LAS char* p = L + L_YT + (c8 * 8 + (i16 >> 2)) * 128 + (g * 16 + (i16 & 3) * 4) * 2;
      const v4u av = trd2(p, p + 512);
      LAS v4u* xp = (LAS v4u*)(L + L_BASE + lane * 128 + ((c8 ^ (lane & 7)) * 16));
      const v4u xv = *xp;
      v4u o;
#pragma unroll
      for (int e = 0; e < 4; ++e) o[e] = pk2(bflo(xv[e]) + bflo(av[e]), bfhi(xv[e]) + bfhi(av[e]));
      *xp = o;
    }
    STAMP(10);
  }
  bar();
  STAMP(11);
  {
    const int tok = tid >> 3, c8 = tid & 7;
    f32x4 s0 = {}, s1 = {};
#pragma unroll
    for (int k = 0; k < 4; ++k) {
      s0 += *(LAS f32x4*)(L + k * WDS + tok * 256 + (((2 * c8) ^ (tok & 15)) * 16));
      s1 += *(LAS f32x4*)(L + k * WDS + tok * 256 + (((2 * c8 + 1) ^ (tok & 15)) * 16));
    }
    const v4u bv = *(LAS v4u*)(L + L_BASE + tok * 128 + ((c8 ^ (tok & 7)) * 16));
    const unsigned f0_ = pk2(s0[0], s0[1]), f1_ = pk2(s0[2], s0[3]), f2_ = pk2(s1[0], s1[1]), f3_ = pk2(s1[2], s1[3]);
    const v4u o = {pk2(bflo(bv.x) + bflo(f0_), bfhi(bv.x) + bfhi(f0_)), pk2(bflo(bv.y) + bflo(f1_), bfhi(bv.y) + bfhi(f1_)),
                   pk2(bflo(bv.z) + bflo(f2_), bfhi(bv.z) + bfhi(f2_)), pk2(bflo(bv.w) + bflo(f3_), bfhi(bv.w) + bfhi(f3_))};
    *(GAS v4u*)((GAS char*)a.out + (size_t)(m2 * 64 + tok) * 1024 + n2 * 128 + c8 * 16) = o;
  }
  asm volatile(".Lfk_end:");
}

extern "C" void kernel_launch(void* const* d_in, const int* in_sizes, int n_in, void* d_out, int out_size, void* d_ws, size_t ws_size,
                              hipStream_t stream) {
  Args a;
  a.x = (const u16*)d_in[0]; a.are = (const u16*)d_in[1]; a.aim = (const u16*)d_in[2]; a.bre = (const u16*)d_in[3]; a.bim = (const u16*)d_in[4];
  a.cre = (const u16*)d_in[5]; a.cim = (const u16*)d_in[6]; a.logdt = (const u16*)d_in[7]; a.dsk = (const u16*)d_in[8];
  a.wg = (const u16*)d_in[10]; a.wu = (const u16*)d_in[11]; a.wd = (const u16*)d_in[12];
  a.out = (u16*)d_out; a.ws = (char*)d_ws;
  (void)hipFuncSetAttribute((const void*)fused_kernel, hipFuncAttributeMaxDynamicSharedMemorySize, L_TOTAL);
  fused_kernel<<<256, 512, L_TOTAL, stream>>>(a);
}
```

```cpp
#define PROBE 1
#define PROBE_W 0
#include <hip/hip_runtime.h>
#include <hip/hip_bf16.h>

typedef unsigned short u16;
typedef unsigned v4u __attribute__((ext_vector_type(4)));
typedef unsigned v2u __attribute__((ext_vector_type(2)));
typedef short s16x4 __attribute__((ext_vector_type(4)));
typedef __bf16 bf16x8 __attribute__((ext_vector_type(8)));
typedef __bf16 bf16x2 __attribute__((ext_vector_type(2)));
typedef float f32x2 __attribute__((ext_vector_type(2)));
typedef float f32x4 __attribute__((ext_vector_type(4)));
typedef float f32x16 __attribute__((ext_vector_type(16)));
#define LAS __attribute__((address_space(3)))
#define GAS __attribute__((address_space(1)))
#define DEVI __device__ __forceinline__

#define STAMPS 0

constexpr int SEQ = 2048, DM = 512, NMODE = 128, FF = 1376;
constexpr int WS_EPOCH = 0;
constexpr int WS_F1 = 4096;
constexpr int WS_F2 = 8192;
constexpr int WS_F3 = 12288;
constexpr int WS_STAT = 16384;
constexpr int WS_STAMP = 65536;
constexpr int WS_XT = 1 << 20;
constexpr int WS_YT = 3 << 20;
constexpr int WS_H = 5 << 20;
constexpr int H_BYTES = 64 * 86 * 1024;
constexpr int WS_END = WS_H + H_BYTES;
constexpr int RS = 41024, WGO = 16384, WUO = 16384 + 12320;
constexpr int CS = 4160;
constexpr int L_KIMG = 3 * RS;
constexpr int L_UIMG = L_KIMG + 4 * CS;
constexpr int US = 4608;
constexpr int L_PRM = L_UIMG + 2 * US;
constexpr int L_KTAB = L_PRM + 4096;
constexpr int KTS = 648 * 4;
constexpr int L_STAT = L_KTAB + 2 * KTS;
constexpr int L_CSUM = L_STAT + 512;
constexpr int L_K2048 = L_CSUM + 1536;
constexpr int L_LNST = L_K2048 + 16;
constexpr int L_CNT = L_LNST + 64;
constexpr int L_TOTAL = L_CNT + 64;
static_assert(L_TOTAL <= 163840, "lds");
constexpr int LNS = 1088;
constexpr int WDS = 30720;
constexpr int L_BASE = L_KIMG;
constexpr int L_YT = L_KIMG + 8192;
constexpr unsigned SPIN_CAP = 1u << 17;

DEVI float bf2f(unsigned b) { return __builtin_bit_cast(float, b << 16); }
DEVI float bflo(unsigned w) { return __builtin_bit_cast(float, w << 16); }
DEVI float bfhi(unsigned w) { return __builtin_bit_cast(float, w & 0xffff0000u); }
DEVI unsigned pk2(float a, float b) { f32x2 v = {a, b}; return __builtin_bit_cast(unsigned, __builtin_convertvector(v, bf16x2)); }
DEVI float rbf(float a) { return bflo(pk2(a, a)); }
DEVI f32x16 mfma32(v4u a, v4u b, f32x16 c) { return __builtin_amdgcn_mfma_f32_32x32x16_bf16(__builtin_bit_cast(bf16x8, a), __builtin_bit_cast(bf16x8, b), c, 0, 0, 0); }
DEVI f32x4 mfma16(v4u a, v4u b, f32x4 c) { return __builtin_amdgcn_mfma_f32_16x16x32_bf16(__builtin_bit_cast(bf16x8, a), __builtin_bit_cast(bf16x8, b), c, 0, 0, 0); }
DEVI v2u trd(LAS char* p) { return __builtin_bit_cast(v2u, __builtin_amdgcn_ds_read_tr16_b64_v4i16((LAS s16x4*)p)); }
DEVI v4u trd2(LAS char* p0, LAS char* p1) { v2u a = trd(p0), b = trd(p1); return v4u{a.x, a.y, b.x, b.y}; }
DEVI void dma16(const void* sbase, unsigned voff, unsigned ldsaddr) {
  asm volatile("s_mov_b32 m0, %2\n\ts_nop 0\n\tglobal_load_lds_dwordx4 %0, %1" :: "v"(voff), "s"(sbase), "s"(ldsaddr) : "memory");
}
template <int N> DEVI void waitv() { asm volatile("s_waitcnt vmcnt(%0)" :: "i"(N) : "memory"); }
DEVI void bar() { asm volatile("s_waitcnt lgkmcnt(0)\n\ts_barrier" ::: "memory"); }
DEVI void sched0() { __builtin_amdgcn_sched_barrier(0); }
DEVI void ld16(v4u& r, const void* sbase, unsigned voff) { asm volatile("global_load_dwordx4 %0, %1, %2" : "=v"(r) : "v"(voff), "s"(sbase) : "memory"); }
DEVI void ld16c(v4u& r, const void* sbase, unsigned voff) { asm volatile("global_load_dwordx4 %0, %1, %2 sc1" : "=v"(r) : "v"(voff), "s"(sbase) : "memory"); }
DEVI void ld4c(unsigned& r, const void* sbase, unsigned voff) { asm volatile("global_load_dword %0, %1, %2 sc1" : "=v"(r) : "v"(voff), "s"(sbase) : "memory"); }
DEVI void ld2(unsigned& r, const void* sbase, unsigned voff) { asm volatile("global_load_ushort %0, %1, %2" : "=v"(r) : "v"(voff), "s"(sbase) : "memory"); }
DEVI void st16c(void* sbase, unsigned voff, v4u v) { asm volatile("global_store_dwordx4 %0, %1, %2 sc1\n\ts_nop 1" :: "v"(voff), "v"(v), "s"(sbase) : "memory"); }
DEVI void st8c(void* sbase, unsigned voff, v2u v) { asm volatile("global_store_dwordx2 %0, %1, %2 sc1\n\ts_nop 1" :: "v"(voff), "v"(v), "s"(sbase) : "memory"); }
DEVI void st4c(void* sbase, unsigned voff, unsigned v) { asm volatile("global_store_dword %0, %1, %2 sc1\n\ts_nop 1" :: "v"(voff), "v"(v), "s"(sbase) : "memory"); }
#define PIN(x) asm volatile("" : "+v"(x))
DEVI float wsum(float v) {
#pragma unroll
  for (int o = 32; o; o >>= 1) v += __shfl_xor(v, o);
  return v;
}
DEVI unsigned lds_add(LAS char* L, int off) { return __hip_atomic_fetch_add((LAS unsigned*)(L + off), 1u, __ATOMIC_RELAXED, __HIP_MEMORY_SCOPE_WORKGROUP); }
DEVI void lds_arrive(LAS char* L, int off, int lane) { asm volatile("s_waitcnt lgkmcnt(0)" ::: "memory"); if (lane == 0) lds_add(L, off); }
DEVI void lds_wait(LAS char* L, int off, unsigned n) {
#pragma unroll 1
  for (unsigned sp = 0; sp < (1u << 24); ++sp) {
    unsigned v = __hip_atomic_load((LAS unsigned*)(L + off), __ATOMIC_RELAXED, __HIP_MEMORY_SCOPE_WORKGROUP);
    if (__builtin_amdgcn_readfirstlane(v) >= n) break;
    __builtin_amdgcn_s_sleep(1);
  }
  asm volatile("" ::: "memory");
}
#define STAMP(i) do { if ((i) == 0 || (i) == PROBE) { unsigned long long t_; asm volatile("s_memrealtime %0\n\ts_waitcnt lgkmcnt(0)" : "=s"(t_) :: "memory"); if ((i) == 0) pr0 = t_; else pr1 = t_; } } while (0)

struct Args {
  const u16 *x, *are, *aim, *bre, *bim, *cre, *cim, *logdt, *dsk, *wg, *wu, *wd;
  u16* out;
  char* ws;
};

__global__ void __launch_bounds__(512, 2) fused_kernel(Args a) {
  extern __shared__ __attribute__((aligned(16))) char smem[];
  LAS char* L = (LAS char*)smem;
  const unsigned lbase = (unsigned)(size_t)smem;
  unsigned long long pr0 = 0, pr1 = 0;
  STAMP(0);
  const int tid = threadIdx.x, lane = tid & 63;
  const int w = __builtin_amdgcn_readfirstlane(tid >> 6);
  const bool helper = w >= 4;
  const int hw = w & 3;
  const int b = blockIdx.x, xcd = b & 7, slot = b >> 3;
  const int m = (xcd >> 2) * 8 + (slot >> 2), j = (xcd & 3) * 4 + (slot & 3);
  const int rot1 = ((slot >> 2) + 2 * (slot & 3)) & 7;
  const int f0 = min(96 * j, FF - 96);
  const int m2 = xcd * 4 + (slot >> 3), n2 = slot & 7;
  const int rot2 = ((n2 * 11) / 4 + ((slot >> 3) * 11) / 2) % 22;
  char* ws = a.ws;

  unsigned codetouch = 0;
  {
    unsigned long long pc; unsigned sz;
    asm volatile("s_getpc_b64 %0\n.Lfk_start:\n\ts_mov_b32 %1, .Lfk_end-.Lfk_start" : "=s"(pc), "=s"(sz));
    unsigned off = (unsigned)((lane * 8 + (slot & 7)) * 128);
    if (w == 3 && off < sz) asm volatile("global_load_dword %0, %1, %2" : "=v"(codetouch) : "v"(off), "s"(pc) : "memory");
  }
  const int t0 = m * 128 + (j >> 1) * 16;
  v4u ra, rb;
  ld16(ra, a.x + (size_t)(t0 + w) * DM, lane * 16);
  ld16(rb, a.x + (size_t)(t0 + w + 8) * DM, lane * 16);
  unsigned p_are, p_aim, p_bre, p_bim, p_cre, p_cim, p_ldt;
  if (!helper) {
    const unsigned po = (unsigned)(b * 256 + tid) * 2;
    ld2(p_are, a.are, po); ld2(p_aim, a.aim, po); ld2(p_bre, a.bre, po); ld2(p_bim, a.bim, po);
    ld2(p_cre, a.cre, po); ld2(p_cim, a.cim, po); ld2(p_ldt, a.logdt, (unsigned)(2 * b + (tid >> 7)) * 2);
  }
  unsigned epoch_v;
  ld4c(epoch_v, ws + WS_EPOCH, 0u);
  if (tid < 16) *(LAS unsigned*)(L + L_CNT + tid * 4) = 0u;
  asm volatile("s_barrier" ::: "memory");

  unsigned vx0, vx1, vw0, vw1, vw2;
  {
    const int r = lane >> 3, cp = lane & 7;
    vx0 = r * 1024 + ((cp ^ (lane >> 4)) * 16);
    vx1 = r * 1024 + ((cp ^ (4 + (lane >> 4))) * 16);
    int n0 = lane, n1 = 64 + lane, n2_ = 128 + lane;
    vw0 = (n0 / 12) * (FF * 2) + (n0 % 12) * 16;
    vw1 = (n1 / 12) * (FF * 2) + (n1 % 12) * 16;
    vw2 = (n2_ / 12) * (FF * 2) + (n2_ % 12) * 16;
  }
  auto issue_stage = [&](int s, int sl) {
    const int ks = (s + rot1) & 7;
    const char* xs = (const char*)a.x + (size_t)(m * 128 + hw * 32) * 1024 + ks * 128;
    const unsigned lb = lbase + sl * RS;
    dma16(xs, vx0, lb + hw * 4096);
    dma16(xs + 8192, vx1, lb + hw * 4096 + 1024);
    dma16(xs + 16384, vx0, lb + hw * 4096 + 2048);
    dma16(xs + 24576, vx1, lb + hw * 4096 + 3072);
    const size_t wo = (size_t)(ks * 64 + hw * 16) * (FF * 2) + f0 * 2;
    const char* gs = (const char*)a.wg + wo;
    const char* us = (const char*)a.wu + wo;
    dma16(gs, vw0, lb + WGO + hw * 3072);
    dma16(gs, vw1, lb + WGO + hw * 3072 + 1024);
    dma16(gs, vw2, lb + WGO + hw * 3072 + 2048);
    dma16(us, vw0, lb + WUO + hw * 3072);
    dma16(us, vw1, lb + WUO + hw * 3072 + 1024);
    dma16(us, vw2, lb + WUO + hw * 3072 + 2048);
  };
  if (helper) { issue_stage(0, 0); issue_stage(1, 1); waitv<20>(); }
  else waitv<0>();
  PIN(ra); PIN(rb); PIN(codetouch); PIN(epoch_v);
  const unsigned epoch = __builtin_amdgcn_readfirstlane(epoch_v);
  const unsigned tag = epoch * 0x9E3779B1u + 0x7F4A7C15u;
  STAMP(1);

  {
    float va[8], vb[8];
#pragma unroll
    for (int i = 0; i < 4; ++i) { va[2 * i] = bflo(ra[i]); va[2 * i + 1] = bfhi(ra[i]); vb[2 * i] = bflo(rb[i]); vb[2 * i + 1] = bfhi(rb[i]); }
    float sa = 0.f, sb = 0.f;
#pragma unroll
    for (int i = 0; i < 8; ++i) { sa += va[i]; sb += vb[i]; }
    sa = wsum(sa) * (1.f / DM); sb = wsum(sb) * (1.f / DM);
    float qa = 0.f, qb = 0.f;
#pragma unroll
    for (int i = 0; i < 8; ++i) { float da = va[i] - sa, db = vb[i] - sb; qa += da * da; qb += db * db; }
    qa = wsum(qa) * (1.f / DM); qb = wsum(qb) * (1.f / DM);
    const float ma = rbf(sa), mb = rbf(sb);
    const float da = rbf(__builtin_sqrtf(rbf(rbf(qa) + 1e-5f))), db = rbf(__builtin_sqrtf(rbf(rbf(qb) + 1e-5f)));
    const float ia = 1.f / da, ib = 1.f / db;
    unsigned oa[4], ob[4];
#pragma unroll
    for (int i = 0; i < 4; ++i) {
      unsigned ca = pk2(va[2 * i] - ma, va[2 * i + 1] - ma), cb = pk2(vb[2 * i] - mb, vb[2 * i + 1] - mb);
      oa[i] = pk2(bflo(ca) * ia, bfhi(ca) * ia); ob[i] = pk2(bflo(cb) * ib, bfhi(cb) * ib);
    }
    if ((lane >> 5) == (j & 1)) {
      LAS char* p = L + L_UIMG + w * LNS + (lane & 31) * 32;
      *(LAS v4u*)p = v4u{oa[0], ob[0], oa[1], ob[1]};
      *(LAS v4u*)(p + 16) = v4u{oa[2], ob[2], oa[3], ob[3]};
    }
    if (lane == 0) {
      *(LAS unsigned*)(L + L_LNST + w * 4) = pk2(ma, da);
      *(LAS unsigned*)(L + L_LNST + 32 + w * 4) = pk2(mb, db);
    }
  }
  if (!helper) {
    PIN(p_are); PIN(p_aim); PIN(p_bre); PIN(p_bim); PIN(p_cre); PIN(p_cim); PIN(p_ldt);
    const float dt = rbf(expf(bf2f(p_ldt)));
    const float ar = (dt * bf2f(p_are)) * 1.44269504088896341f;
    const float ai = (dt * bf2f(p_aim)) * 0.15915494309189535f;
    const float br = dt * bf2f(p_bre), bi = dt * bf2f(p_bim), cr0 = bf2f(p_cre), ci0 = bf2f(p_cim);
    const float cr = cr0 * br - ci0 * bi, ci = cr0 * bi + ci0 * br;
    *(LAS f32x4*)(L + L_PRM + tid * 16) = f32x4{ar, ai, cr, ci};
    const float mag = __builtin_amdgcn_exp2f(ar * 2048.f), th = __builtin_amdgcn_fractf(ai * 2048.f);
    const float kv = wsum(mag * (cr * __builtin_amdgcn_cosf(th) - ci * __builtin_amdgcn_sinf(th)));
    if (lane == 0) *(LAS float*)(L + L_K2048 + w * 4) = kv;
  }
  bar();
  STAMP(2);
  if (!helper) {
#pragma unroll
    for (int it = 0; it < 2; ++it) {
      const int p0 = (w * 2 + it) * 16 + (lane >> 4) * 4;
      LAS char* p = L + L_UIMG + ((lane & 15) >> 2) * LNS + (p0 + (lane & 3)) * 8;
      const v4u v = trd2(p, p + 4 * LNS);
      const int pair = (j & 1) * 128 + p0 + ((lane & 15) >> 2);
      st16c(ws + WS_XT, (unsigned)(pair * 8192 + (t0 >> 4) * 64 + (lane & 3) * 16), v);
    }
    if (w == 0 && !(j & 1) && lane < 16) {
      const unsigned sv = *(LAS unsigned*)(L + L_LNST + lane * 4);
      st8c(ws + WS_STAT, (unsigned)(t0 + lane) * 8, v2u{sv, tag});
    }
  }
  {
    const int cl = w >> 2, g = lane >> 4;
    const float fi = (float)(lane & 15);
    f32x4 d1 = {}, d2 = {}, d4 = {}, d8 = {};
#pragma unroll 1
    for (int kk = 0; kk < 2; ++kk) {
      v4u fa[4], fb[4];
#pragma unroll
      for (int e = 0; e < 4; ++e) {
        const f32x4 p = *(LAS f32x4*)(L + L_PRM + (cl * 128 + (hw * 2 + kk) * 16 + g * 4 + e) * 16);
        const float mag = __builtin_amdgcn_exp2f(p.x * fi), th = __builtin_amdgcn_fractf(p.y * fi);
        float zr = mag * __builtin_amdgcn_cosf(th), zi = mag * __builtin_amdgcn_sinf(th);
#pragma unroll
        for (int s = 0; s < 4; ++s) {
          fa[s][e] = pk2(zr, zi);
          const float t = zr * zi; zr = zr * zr - zi * zi; zi = t + t;
        }
#pragma unroll
        for (int s = 0; s < 4; ++s) {
          fb[s][e] = pk2(p.z * zr - p.w * zi, -(p.z * zi) - p.w * zr);
          if (s < 3) { const float t = zr * zi; zr = zr * zr - zi * zi; zi = t + t; }
        }
      }
      d1 = mfma16(fa[0], fb[0], d1); d2 = mfma16(fa[1], fb[1], d2); d4 = mfma16(fa[2], fb[2], d4); d8 = mfma16(fa[3], fb[3], d8);
    }
    LAS char* pp = L + 2 * RS + w * 2560 + ((lane & 15) * 16 + g * 4) * 4;
    *(LAS f32x4*)pp = d1;
    if (lane & 8) { *(LAS f32x4*)(pp + 512) = d2; *(LAS f32x4*)(pp + 1024) = d4; *(LAS f32x4*)(pp + 1536) = d8; }
  }
  bar();
  if (tid < 320) {
    const int cl = tid >= 160, q = tid - cl * 160;
    LAS char* pp = L + 2 * RS + cl * 10240 + q * 16;
    const f32x4 s = (*(LAS f32x4*)pp + *(LAS f32x4*)(pp + 2560)) + (*(LAS f32x4*)(pp + 5120) + *(LAS f32x4*)(pp + 7680));
    *(LAS f32x4*)(L + L_KTAB + cl * KTS + q * 16) = s;
  } else if (tid < 322) {
    const int cl = tid - 320;
    *(LAS float*)(L + L_KTAB + cl * KTS + 2560) = *(LAS float*)(L + L_K2048 + cl * 8) + *(LAS float*)(L + L_K2048 + cl * 8 + 4);
  }
  STAMP(3);

  if (!helper) {
    const int tg = w & 1, fh = w >> 1;
    const int r = lane & 31, h = lane >> 5;
    f32x16 acc[2][3];
#pragma unroll
    for (int i = 0; i < 2; ++i)
#pragma unroll
      for (int c = 0; c < 3; ++c) acc[i][c] = f32x16{};
    f32x4 cs[3] = {};
    const unsigned one2 = ((lane & 15) == ((lane >> 4) & 1)) ? 0x3F803F80u : 0u;
    const v4u sel = {one2, one2, one2, one2};
    int xo[4];
#pragma unroll
    for (int ks = 0; ks < 4; ++ks) xo[ks] = (tg * 64 + r) * 128 + (((2 * ks + h) ^ ((r >> 1) & 7)) * 16);
    const int wo = WGO + ((lane >> 4) & 1) * 12320 + (8 * h + ((lane & 15) >> 2)) * 192 + fh * 96 + (lane & 3) * 8;
    v4u xf[2][2], wf[2][3];
    auto ldf = [&](int buf, LAS char* S, int ks) {
      xf[buf][0] = *(LAS v4u*)(S + xo[ks]);
      xf[buf][1] = *(LAS v4u*)(S + xo[ks] + 4096);
#pragma unroll
      for (int c = 0; c < 3; ++c) wf[buf][c] = trd2(S + wo + ks * 3072 + c * 32, S + wo + ks * 3072 + 768 + c * 32);
    };
    auto mm = [&](int buf) {
#pragma unroll
      for (int c = 0; c < 3; ++c) { acc[0][c] = mfma32(wf[buf][c], xf[buf][0], acc[0][c]); acc[1][c] = mfma32(wf[buf][c], xf[buf][1], acc[1][c]); }
    };
    auto msel = [&](int buf) {
#pragma unroll
      for (int c = 0; c < 3; ++c) cs[c] = mfma16(sel, wf[buf][c], cs[c]);
    };
    bar();
    STAMP(4);
    int sl = 0;
    ldf(0, L, 0);
#pragma unroll 1
    for (int s = 0; s < 8; ++s) {
      LAS char* S = L + sl * RS;
      ldf(1, S, 1); sched0();
      mm(0); if (tg == 0) msel(0); sched0();
      ldf(0, S, 2); sched0();
      mm(1); if (tg == 1) msel(1); sched0();
      ldf(1, S, 3); sched0();
      mm(0); if (tg == 0) msel(0); sched0();
      if (tg == 1) msel(1);
      if (s == 7 && lane < 16) {
#pragma unroll
        for (int c = 0; c < 3; ++c) {
          *(LAS float*)(L + L_CSUM + tg * 768 + ((fh * 3 + c) * 32 + lane) * 4) = cs[c][0];
          *(LAS float*)(L + L_CSUM + tg * 768 + ((fh * 3 + c) * 32 + 16 + lane) * 4) = cs[c][1];
        }
      }
      if (s == 0) waitv<0>();
      sched0();
      bar();
      if (s == 0 && tid == 0) st4c(ws + WS_F1, (unsigned)b * 4, tag);
      sl = sl == 2 ? 0 : sl + 1;
      if (s < 7) ldf(0, L + sl * RS, 0);
      sched0();
      mm(1); sched0();
    }
    STAMP(5);
    float c1[2], ir2[2];
    {
      v4u xa[2];
#pragma unroll
      for (int i = 0; i < 2; ++i) {
        const unsigned sv = *(LAS unsigned*)(L + L_STAT + (tg * 64 + i * 32 + r) * 4);
        const unsigned nm = (sv & 0xffffu) ^ 0x8000u;
        xa[i] = h ? v4u{0u, 0u, 0u, 0u} : v4u{nm | (nm << 16), nm, 0u, 0u};
        const float sd = bfhi(sv);
        c1[i] = -1.44269504088896341f / sd; ir2[i] = sd * sd;
      }
#pragma unroll
      for (int c = 0; c < 3; ++c) {
        const int o = L_CSUM + ((fh * 3 + c) * 32 + r) * 4;
        const float s = *(LAS float*)(L + o) + *(LAS float*)(L + o + 768);
        const float hi = rbf(s), mid = rbf(s - hi), lo = (s - hi) - mid;
        const v4u wa = h ? v4u{0u, 0u, 0u, 0u} : v4u{pk2(hi, mid), pk2(lo, 0.f), 0u, 0u};
        acc[0][c] = mfma32(wa, xa[0], acc[0][c]); acc[1][c] = mfma32(wa, xa[1], acc[1][c]);
      }
    }
#pragma unroll
    for (int c = 0; c < 3; ++c) {
      const int F16 = f0 / 16 + fh * 3 + c;
#pragma unroll
      for (int i = 0; i < 2; ++i) {
        float hv[8];
#pragma unroll
        for (int e = 0; e < 8; ++e) {
          const float g = acc[i][c][e], u = acc[i][c][e + 8];
          const float ex = __builtin_amdgcn_exp2f(g * c1[i]);
          hv[e] = (g * u) * __builtin_amdgcn_rcpf(ex * ir2[i] + ir2[i]);
        }
        const v4u o = {pk2(hv[0], hv[1]), pk2(hv[2], hv[3]), pk2(hv[4], hv[5]), pk2(hv[6], hv[7])};
        if (F16 >= 6 * j) st16c(ws + WS_H + (size_t)((m * 4 + tg * 2 + i) * 86 + F16) * 1024, lane * 16, o);
      }
    }
    STAMP(6);
    waitv<0>();
    if (lane == 0) { if (lds_add(L, L_CNT + 0) == 3u && j < 15) st4c(ws + WS_F2, (unsigned)(m * 128 + j * 4), tag); }
    STAMP(7);
    const int kq = w;
    LAS char* W = L + kq * WDS;
    const int q = (lane & 15) >> 2;
    const int wa0 = (4 * h + q) * 128 + (q >> 1) * 64 + ((lane >> 4) & 1) * 32 + (lane & 3) * 8;
    const int wa1 = wa0 ^ 64;
    const unsigned vd = (lane >> 3) * 1024 + (((lane & 7) ^ (((lane >> 4) & 1) << 2)) * 16);
    lds_wait(L, L_CNT + 16 + kq * 4, 1u);
    v4u we[7][2];
#pragma unroll
    for (int i = 0; i < 7; ++i) { we[i][0] = trd2(W + i * 2048 + wa0, W + i * 2048 + 1024 + wa0); we[i][1] = trd2(W + i * 2048 + wa1, W + i * 2048 + 1024 + wa1); }
#pragma unroll
    for (int i = 0; i < 7; ++i) { PIN(we[i][0]); PIN(we[i][1]); }
    asm volatile("s_waitcnt lgkmcnt(0)" ::: "memory");
#pragma unroll
    for (int i = 15; i < 22; ++i) {
      int ii = i + rot2; ii = ii >= 22 ? ii - 22 : ii;
      const int F16 = min(22 * kq + ii, 85);
      const char* src = (const char*)a.wd + (size_t)F16 * 16384 + n2 * 128;
      dma16(src, vd, lbase + kq * WDS + (i - 15) * 2048);
      dma16(src + 8192, vd, lbase + kq * WDS + (i - 15) * 2048 + 1024);
    }
    STAMP(8);
    {
      const int jlo = (22 * kq) / 6, jhi = min(22 * kq + 21, 85) / 6;
      const bool need = lane >= jlo && lane <= jhi;
      _Pragma("unroll 1") for (unsigned sp = 0; sp < SPIN_CAP; ++sp) {
        unsigned fv; ld4c(fv, ws + WS_F2 + (m2 >> 1) * 128, (unsigned)(lane & 15) * 4);
        waitv<0>(); PIN(fv);
        if (__all(!need || fv == tag)) break;
        __builtin_amdgcn_s_sleep(2);
      }
    }
    STAMP(9);
    const __amdgpu_buffer_rsrc_t rh = __builtin_amdgcn_make_buffer_rsrc((void*)(ws + WS_H), 0, H_BYTES, 0x00020000);
    constexpr int HW = 8;
    v4u hf[HW][2];
    auto ldh = [&](int i) {
      int ii = i + rot2; ii = ii >= 22 ? ii - 22 : ii;
      const int F16 = 22 * kq + ii;
      const int so = F16 < 86 ? (m2 * 2 * 86 + F16) * 1024 : 0x40000000;
      hf[i % HW][0] = __builtin_amdgcn_raw_buffer_load_b128(rh, lane * 16, so, 16);
      hf[i % HW][1] = __builtin_amdgcn_raw_buffer_load_b128(rh, lane * 16, so + 86 * 1024, 16);
    };
#pragma unroll
    for (int i = 0; i < HW; ++i) ldh(i);
    sched0();
    f32x16 o2[2][2];
#pragma unroll
    for (int i = 0; i < 4; ++i) o2[i >> 1][i & 1] = f32x16{};
#pragma unroll
    for (int i = 0; i < 22; ++i) {
      v4u w0, w1;
      if (i < 7) { w0 = we[i][0]; w1 = we[i][1]; }
      else { const int pos = (i < 15 ? i : i - 15) * 2048; w0 = trd2(W + pos + wa0, W + pos + 1024 + wa0); w1 = trd2(W + pos + wa1, W + pos + 1024 + wa1); }
      const v4u h0 = hf[i % HW][0], h1 = hf[i % HW][1];
      o2[0][0] = mfma32(w0, h0, o2[0][0]); o2[1][0] = mfma32(w1, h0, o2[1][0]);
      o2[0][1] = mfma32(w0, h1, o2[0][1]); o2[1][1] = mfma32(w1, h1, o2[1][1]);
      if (i + HW < 22) ldh(i + HW);
      sched0();
    }
    STAMP(10);
#pragma unroll
    for (int cc = 0; cc < 2; ++cc)
#pragma unroll
      for (int tt = 0; tt < 2; ++tt)
#pragma unroll
        for (int g4 = 0; g4 < 4; ++g4) {
          const int tok = tt * 32 + r, ch4 = cc * 8 + g4 * 2 + h;
          *(LAS f32x4*)(W + tok * 256 + ((ch4 ^ (tok & 15)) * 16)) = f32x4{o2[cc][tt][4 * g4], o2[cc][tt][4 * g4 + 1], o2[cc][tt][4 * g4 + 2], o2[cc][tt][4 * g4 + 3]};
        }
  } else {
    const int ht = tid - 256;
    v4u pst, pfl;
    int sl = 0;
#pragma unroll 1
    for (int s = 0; s < 6; ++s) {
      waitv<10>();
      bar();
      issue_stage(s + 2, sl == 0 ? 2 : sl - 1);
      sl = sl == 2 ? 0 : sl + 1;
      if (s == 0) {
#pragma unroll 1
        for (int id = ht; id < 520; id += 256) {
          const int cl = id >= 260, m0 = (id - cl * 260) * 8;
          float v[9];
#pragma unroll
          for (int e = 0; e < 9; ++e) {
            const int lag = 2047 - m0 - e;
            int big = (int)(pk2((float)lag, 0.f) & 0xffffu) - 0x4300 + 128;
            PIN(big);
            const int idx = lag < 128 ? max(lag, 0) : big;
            const float kv = *(LAS float*)(L + L_KTAB + cl * KTS + idx * 4);
            v[e] = lag < 0 ? 0.f : kv;
          }
          *(LAS v4u*)(L + L_KIMG + cl * 2 * CS + m0 * 2) = v4u{pk2(v[0], v[1]), pk2(v[2], v[3]), pk2(v[4], v[5]), pk2(v[6], v[7])};
          *(LAS v4u*)(L + L_KIMG + cl * 2 * CS + CS + m0 * 2) = v4u{pk2(v[1], v[2]), pk2(v[3], v[4]), pk2(v[5], v[6]), pk2(v[7], v[8])};
        }
        if (ht < 64) *(LAS v4u*)(L + L_UIMG + (ht >> 5) * US + (ht & 31) * 16) = v4u{0u, 0u, 0u, 0u};
      }
    }
    ld16c(pst, ws + WS_STAT + m * 1024, lane * 16); ld16c(pfl, ws + WS_F1, lane * 16);
    waitv<12>(); bar();
    waitv<2>(); bar();
    STAMP(4);
    waitv<0>(); PIN(pst); PIN(pfl);
    _Pragma("unroll 1") for (unsigned sp = 0; sp < SPIN_CAP; ++sp) {
      if (__all(pst.y == tag && pst.w == tag)) break;
      __builtin_amdgcn_s_sleep(2);
      ld16c(pst, ws + WS_STAT + m * 1024, lane * 16); waitv<0>(); PIN(pst);
    }
    if (hw == 0) *(LAS v2u*)(L + L_STAT + lane * 8) = v2u{pst.x, pst.z};
    v4u u0, u1;
    const bool e1 = __all(pfl.x == tag && pfl.y == tag && pfl.z == tag && pfl.w == tag);
    if (e1) { ld16c(u0, ws + WS_XT + b * 8192, ht * 16); ld16c(u1, ws + WS_XT + b * 8192 + 4096, ht * 16); }
    bar();
    if (!e1) {
      _Pragma("unroll 1") for (unsigned sp = 0; sp < SPIN_CAP; ++sp) {
        __builtin_amdgcn_s_sleep(2);
        ld16c(pfl, ws + WS_F1, lane * 16); waitv<0>(); PIN(pfl);
        if (__all(pfl.x == tag && pfl.y == tag && pfl.z == tag && pfl.w == tag)) break;
      }
      ld16c(u0, ws + WS_XT + b * 8192, ht * 16); ld16c(u1, ws + WS_XT + b * 8192 + 4096, ht * 16);
    }
    STAMP(5);
    const unsigned vd = (lane >> 3) * 1024 + (((lane & 7) ^ (((lane >> 4) & 1) << 2)) * 16);
    auto issue_wd = [&](int i) {
      int ii = i + rot2; ii = ii >= 22 ? ii - 22 : ii;
      const int F16 = min(22 * hw + ii, 85);
      const char* src = (const char*)a.wd + (size_t)F16 * 16384 + n2 * 128;
      dma16(src, vd, lbase + hw * WDS + i * 2048);
      dma16(src + 8192, vd, lbase + hw * WDS + i * 2048 + 1024);
    };
    constexpr int WDE = 4;
#pragma unroll
    for (int i = 0; i < WDE; ++i) issue_wd(i);
    waitv<2 * WDE>(); PIN(u0); PIN(u1);
    if (b == 0 && ht == 0) st4c(ws + WS_EPOCH, 0u, epoch + 1u);
    {
      const int sub = ht & 3, g = ht >> 2;
      LAS char* p = L + L_UIMG + (sub & 1) * US + 512 + (g * 16 + (sub >> 1) * 8) * 2;
      *(LAS v4u*)p = u0;
      *(LAS v4u*)(p + 2048) = u1;
    }
    lds_arrive(L, L_CNT + 4, lane);
#pragma unroll
    for (int i = WDE; i < 15; ++i) issue_wd(i);
    lds_wait(L, L_CNT + 4, 4u);
    STAMP(6);
    const int cl = hw >> 1, rh_ = hw & 1, i16 = lane & 15, g = lane >> 4;
    f32x4 y[8];
#pragma unroll
    for (int t = 0; t < 8; ++t) y[t] = f32x4{};
    {
      const int s0 = 2031 - i16 + 8 * g;
      LAS char* ka = L + L_KIMG + cl * 2 * CS + (s0 & 1) * CS + (s0 >> 1) * 4;
      LAS char* ub = L + L_UIMG + cl * US + 512 + (16 * i16 - 16 + 8 * g) * 2;
#pragma unroll 1
      for (int rr = 0; rr < 4; ++rr) {
        const int r = rh_ * 4 + rr;
        v4u fa[8], fb[8];
#pragma unroll
        for (int q = 0; q < 8; ++q) {
          LAS unsigned* p = (LAS unsigned*)(ka - q * 512 - r * 64);
          fa[q] = v4u{p[0], p[1], p[2], p[3]};
          fb[q] = *(LAS v4u*)(ub + q * 512 - r * 64);
        }
#pragma unroll
        for (int q = 0; q < 8; ++q)
#pragma unroll
          for (int t = q; t < 8; ++t) y[t] = mfma16(fa[q], fb[t - q], y[t]);
        if (rr == 1) { waitv<0>(); if (lane == 0) lds_add(L, L_CNT + 16 + hw * 4); }
      }
    }
    STAMP(7);
    lds_arrive(L, L_CNT + 8, lane);
    lds_wait(L, L_CNT + 8, 4u);
    if (rh_) {
#pragma unroll
      for (int t = 0; t < 4; ++t) { const f32x4 tmp = y[t]; y[t] = y[t + 4]; y[t + 4] = tmp; PIN(y[t]); PIN(y[t + 4]); }
    }
#pragma unroll
    for (int t = 0; t < 4; ++t) *(LAS f32x4*)(L + L_KIMG + hw * 4096 + t * 1024 + lane * 16) = y[t + 4];
    lds_arrive(L, L_CNT + 12, lane);
    const float dk = bf2f(a.dsk[2 * b + cl]);
    lds_wait(L, L_CNT + 12, 4u);
#pragma unroll
    for (int t = 0; t < 4; ++t) {
      const f32x4 o = *(LAS f32x4*)(L + L_KIMG + (hw ^ 1) * 4096 + t * 1024 + lane * 16);
      const f32x4 s = y[t] + o;
      LAS v2u* up = (LAS v2u*)(L + L_UIMG + cl * US + 512 + ((rh_ * 4 + t) * 256 + i16 * 16 + g * 4) * 2);
      const v2u uv = *up;
      const unsigned y01 = pk2(s[0], s[1]), y23 = pk2(s[2], s[3]);
      const unsigned k01 = pk2(bflo(uv.x) * dk, bfhi(uv.x) * dk), k23 = pk2(bflo(uv.y) * dk, bfhi(uv.y) * dk);
      *up = v2u{pk2(bflo(y01) + bflo(k01), bfhi(y01) + bfhi(k01)), pk2(bflo(y23) + bflo(k23), bfhi(y23) + bfhi(k23))};
    }
    asm volatile("s_waitcnt lgkmcnt(0)" ::: "memory");
#pragma unroll
    for (int it = 0; it < 2; ++it) {
      const int t = rh_ * 1024 + it * 512 + lane * 8;
      const v4u v = *(LAS v4u*)(L + L_UIMG + cl * US + 512 + t * 2);
      const int ch = 2 * b + cl;
      st16c(ws + WS_YT, (unsigned)((((t >> 6) * 8 + (ch >> 6)) * 64 + (ch & 63)) * 128 + (t & 63) * 2), v);
    }
    waitv<0>();
    if (lane == 0) { if (lds_add(L, L_CNT + 32) == 3u) st4c(ws + WS_F3, (unsigned)b * 4, tag); }
    STAMP(8);
    v4u x0, x1;
    ld16(x0, a.x + (size_t)(m2 * 64) * DM + n2 * 64, (unsigned)((ht >> 3) * 1024 + (ht & 7) * 16));
    ld16(x1, a.x + (size_t)(m2 * 64 + 32) * DM + n2 * 64, (unsigned)((ht >> 3) * 1024 + (ht & 7) * 16));
    _Pragma("unroll 1") for (unsigned sp = 0; sp < SPIN_CAP; ++sp) {
      unsigned fv; ld4c(fv, ws + WS_F3 + n2 * 128, (unsigned)(lane & 31) * 4);
      waitv<0>(); PIN(fv);
      if (__all(fv == tag)) break;
      __builtin_amdgcn_s_sleep(8);
    }
    PIN(x0); PIN(x1);
    STAMP(9);
    v4u a0, a1;
    ld16c(a0, ws + WS_YT + (m2 * 8 + n2) * 8192, ht * 16); ld16c(a1, ws + WS_YT + (m2 * 8 + n2) * 8192 + 4096, ht * 16);
    lds_wait(L, L_CNT + 32, 4u);
    *(LAS v4u*)(L + L_BASE + (ht >> 3) * 128 + (((ht & 7) ^ ((ht >> 3) & 7)) * 16)) = x0;
    *(LAS v4u*)(L + L_BASE + 4096 + (ht >> 3) * 128 + (((ht & 7) ^ ((ht >> 3) & 7)) * 16)) = x1;
    waitv<0>(); PIN(a0); PIN(a1);
    *(LAS v4u*)(L + L_YT + ht * 16) = a0;
    *(LAS v4u*)(L + L_YT + 4096 + ht * 16) = a1;
    lds_arrive(L, L_CNT + 36, lane);
    lds_wait(L, L_CNT + 36, 4u);
#pragma unroll
    for (int it = 0; it < 2; ++it) {
      const int c8 = hw * 2 + it;
      LAS char* p = L + L_YT + (c8 * 8 + (i16 >> 2)) * 128 + (g * 16 + (i16 & 3) * 4) * 2;
      const v4u av = trd2(p, p + 512);
      LAS v4u* xp = (LAS v4u*)(L + L_BASE + lane * 128 + ((c8 ^ (lane & 7)) * 16));
      const v4u xv = *xp;
      v4u o;
#pragma unroll
      for (int e = 0; e < 4; ++e) o[e] = pk2(bflo(xv[e]) + bflo(av[e]), bfhi(xv[e]) + bfhi(av[e]));
      *xp = o;
    }
    STAMP(10);
  }
  bar();
  STAMP(11);
  {
    const int tok = tid >> 3, c8 = tid & 7;
    f32x4 s0 = {}, s1 = {};
#pragma unroll
    for (int k = 0; k < 4; ++k) {
      s0 += *(LAS f32x4*)(L + k * WDS + tok * 256 + (((2 * c8) ^ (tok & 15)) * 16));
      s1 += *(LAS f32x4*)(L + k * WDS + tok * 256 + (((2 * c8 + 1) ^ (tok & 15)) * 16));
    }
    const v4u bv = *(LAS v4u*)(L + L_BASE + tok * 128 + ((c8 ^ (tok & 7)) * 16));
    const unsigned f0_ = pk2(s0[0], s0[1]), f1_ = pk2(s0[2], s0[3]), f2_ = pk2(s1[0], s1[1]), f3_ = pk2(s1[2], s1[3]);
    const v4u o = {pk2(bflo(bv.x) + bflo(f0_), bfhi(bv.x) + bfhi(f0_)), pk2(bflo(bv.y) + bflo(f1_), bfhi(bv.y) + bfhi(f1_)),
                   pk2(bflo(bv.z) + bflo(f2_), bfhi(bv.z) + bfhi(f2_)), pk2(bflo(bv.w) + bflo(f3_), bfhi(bv.w) + bfhi(f3_))};
    *(GAS v4u*)((GAS char*)a.out + (size_t)(m2 * 64 + tok) * 1024 + n2 * 128 + c8 * 16) = o;
  }
  waitv<0>();
  STAMP(12);
  if (w == PROBE_W) {
    const unsigned long long te = __builtin_amdgcn_s_memrealtime() + 10ull * (pr1 - pr0);
    _Pragma("unroll 1") for (int sp = 0; sp < 1000000; ++sp) { if (__builtin_amdgcn_s_memrealtime() >= te) break; __builtin_amdgcn_s_sleep(4); }
  }
  asm volatile(".Lfk_end:");
}

extern "C" void kernel_launch(void* const* d_in, const int* in_sizes, int n_in, void* d_out, int out_size, void* d_ws, size_t ws_size,
                              hipStream_t stream) {
  Args a;
  a.x = (const u16*)d_in[0]; a.are = (const u16*)d_in[1]; a.aim = (const u16*)d_in[2]; a.bre = (const u16*)d_in[3]; a.bim = (const u16*)d_in[4];
  a.cre = (const u16*)d_in[5]; a.cim = (const u16*)d_in[6]; a.logdt = (const u16*)d_in[7]; a.dsk = (const u16*)d_in[8];
  a.wg = (const u16*)d_in[10]; a.wu = (const u16*)d_in[11]; a.wd = (const u16*)d_in[12];
  a.out = (u16*)d_out; a.ws = (char*)d_ws;
  (void)hipFuncSetAttribute((const void*)fused_kernel, hipFuncAttributeMaxDynamicSharedMemorySize, L_TOTAL);
  fused_kernel<<<256, 512, L_TOTAL, stream>>>(a);
}
```

```cpp
#include <hip/hip_runtime.h>
#include <hip/hip_bf16.h>

typedef unsigned short u16;
typedef unsigned v4u __attribute__((ext_vector_type(4)));
typedef unsigned v2u __attribute__((ext_vector_type(2)));
typedef short s16x4 __attribute__((ext_vector_type(4)));
typedef __bf16 bf16x8 __attribute__((ext_vector_type(8)));
typedef __bf16 bf16x2 __attribute__((ext_vector_type(2)));
typedef float f32x2 __attribute__((ext_vector_type(2)));
typedef float f32x4 __attribute__((ext_vector_type(4)));
typedef float f32x16 __attribute__((ext_vector_type(16)));
#define LAS __attribute__((address_space(3)))
#define DEVI __device__ __forceinline__

constexpr int DM = 512, FF = 1376;
constexpr int WS_EPOCH = 0;
constexpr int WS_F1 = 4096;
constexpr int WS_F2 = 1 << 18;
constexpr int WS_STAT = 16384;
constexpr int WS_XT = 1 << 20;
constexpr int WS_YT = 3 << 20;
constexpr int WS_H = 7 << 20;
constexpr int H_BYTES = 64 * 86 * 1024;
constexpr int RS = 41024, WGO = 16384, WUO = 16384 + 12320;
constexpr int CS = 4160;
constexpr int L_KIMG = 3 * RS;
constexpr int L_UIMG = L_KIMG + 4 * CS;
constexpr int US = 4608;
constexpr int L_ZT = L_KIMG, ZTS = 10064;
constexpr int L_CSUM = L_KIMG + 36864;
constexpr int L_PROG = L_ZT + 4 * ZTS;
constexpr int L_TOTAL = L_PROG + 64;
static_assert(L_TOTAL <= 163840, "lds");
DEVI int part_off(int w) { return L_ZT + (w == 2 ? 25856 : w * ZTS); }
constexpr int LNS = 1088;
constexpr int L_STAT = L_KIMG + 34304;
constexpr int L_BASE = L_KIMG + 25856;
constexpr unsigned SPIN_CAP = 1u << 17;
constexpr int PACE = 3, CAP = 12;
constexpr int WWIN = 4;
constexpr int HW = 8, HW0 = 4, TAILN = 6;
constexpr int P_TAG = 32, P_MARK = 36, YAT = 12;
constexpr int wd_pos(int i) { return ((i / 5 + 2) % 3) * RS + (i % 5) * 8192; }

DEVI float bf2f(unsigned b) { return __builtin_bit_cast(float, b << 16); }
DEVI float bflo(unsigned w) { return __builtin_bit_cast(float, w << 16); }
DEVI float bfhi(unsigned w) { return __builtin_bit_cast(float, w & 0xffff0000u); }
DEVI unsigned pk2v(f32x2 v) { return __builtin_bit_cast(unsigned, __builtin_convertvector(v, bf16x2)); }
DEVI unsigned pk2(float a, float b) { return pk2v(f32x2{a, b}); }
DEVI float rbf(float a) { return bflo(pk2(a, a)); }
DEVI f32x16 mfma32(v4u a, v4u b, f32x16 c) { return __builtin_amdgcn_mfma_f32_32x32x16_bf16(__builtin_bit_cast(bf16x8, a), __builtin_bit_cast(bf16x8, b), c, 0, 0, 0); }
DEVI f32x4 mfma16(v4u a, v4u b, f32x4 c) { return __builtin_amdgcn_mfma_f32_16x16x32_bf16(__builtin_bit_cast(bf16x8, a), __builtin_bit_cast(bf16x8, b), c, 0, 0, 0); }
DEVI v2u trd(LAS char* p) { return __builtin_bit_cast(v2u, __builtin_amdgcn_ds_read_tr16_b64_v4i16((LAS s16x4*)p)); }
DEVI v4u trd2(LAS char* p0, LAS char* p1) { v2u a = trd(p0), b = trd(p1); return v4u{a.x, a.y, b.x, b.y}; }
template <int N> DEVI void waitv() { asm volatile("s_waitcnt vmcnt(%0)" :: "i"(N) : "memory"); }
DEVI void waitl() { asm volatile("" ::: "memory"); __builtin_amdgcn_s_waitcnt(0xC07F); asm volatile("" ::: "memory"); }
DEVI void bar() { waitl(); asm volatile("s_waitcnt lgkmcnt(0)\n\ts_barrier" ::: "memory"); }
DEVI void sched0() { __builtin_amdgcn_sched_barrier(0); }
#define SGB(mask, n) __builtin_amdgcn_sched_group_barrier(mask, n, 0)
DEVI void ld16(v4u& r, const void* sbase, unsigned voff) { asm volatile("global_load_dwordx4 %0, %1, %2" : "=v"(r) : "v"(voff), "s"(sbase) : "memory"); }
DEVI void ld16c(v4u& r, const void* sbase, unsigned voff) { asm volatile("global_load_dwordx4 %0, %1, %2 sc1" : "=v"(r) : "v"(voff), "s"(sbase) : "memory"); }
DEVI void ld4c(unsigned& r, const void* sbase, unsigned voff) { asm volatile("global_load_dword %0, %1, %2 sc1" : "=v"(r) : "v"(voff), "s"(sbase) : "memory"); }
DEVI void ld2(unsigned& r, const void* sbase, unsigned voff) { asm volatile("global_load_ushort %0, %1, %2" : "=v"(r) : "v"(voff), "s"(sbase) : "memory"); }
DEVI void st16c(void* sbase, unsigned voff, v4u v) { asm volatile("global_store_dwordx4 %0, %1, %2 sc1\n\ts_nop 1" :: "v"(voff), "v"(v), "s"(sbase) : "memory"); }
DEVI void st8c(void* sbase, unsigned voff, v2u v) { asm volatile("global_store_dwordx2 %0, %1, %2 sc1\n\ts_nop 1" :: "v"(voff), "v"(v), "s"(sbase) : "memory"); }
DEVI void st4c(void* sbase, unsigned voff, unsigned v) { asm volatile("global_store_dword %0, %1, %2 sc1\n\ts_nop 1" :: "v"(voff), "v"(v), "s"(sbase) : "memory"); }
DEVI void flag1(void* sbase, unsigned voff, unsigned v) {
  asm volatile("s_mov_b64 exec, 1\n\tglobal_store_dword %0, %1, %2 sc1\n\ts_mov_b64 exec, -1" :: "v"(voff), "v"(v), "s"(sbase) : "memory");
}
#define PIN(x) asm volatile("" : "+v"(x))
template <int C> DEVI float dppf(float v) { return __builtin_bit_cast(float, __builtin_amdgcn_update_dpp(0, __builtin_bit_cast(int, v), C, 0xf, 0xf, true)); }
DEVI float rsum16(float v) { v += dppf<0xB1>(v); v += dppf<0x4E>(v); v += dppf<0x141>(v); v += dppf<0x140>(v); return v; }
DEVI float cut(float x) { asm("" : "+v"(x)); return x; }
DEVI unsigned pk2s(float a, float b) { return pk2(cut(a), cut(b)); }
DEVI float dot2(unsigned a, unsigned b, float c) { return __builtin_amdgcn_fdot2_f32_bf16(__builtin_bit_cast(bf16x2, a), __builtin_bit_cast(bf16x2, b), c, false); }
DEVI void dma2(const void* sbase, unsigned v0, unsigned v1, unsigned ldsaddr) {
  asm volatile("s_mov_b32 m0, %3\n\ts_nop 0\n\tglobal_load_lds_dwordx4 %0, %2\n\tglobal_load_lds_dwordx4 %1, %2 offset:1024" :: "v"(v0), "v"(v1), "s"(sbase), "s"(ldsaddr) : "memory");
}
#define DMA4(a, b, c, d, sb, lb, off, p, q) \
  "s_waitcnt vmcnt(%" #q ")\n\ts_add_i32 m0, %" #lb ", " #off "\n\ts_nop %" #p "\n\tglobal_load_lds_dwordx4 %" #a ", %" #sb "\n\ts_nop %" #p "\n\tglobal_load_lds_dwordx4 %" #b ", %" #sb " offset:1024\n\t" \
  "s_nop %" #p "\n\tglobal_load_lds_dwordx4 %" #c ", %" #sb " offset:2048\n\ts_nop %" #p "\n\tglobal_load_lds_dwordx4 %" #d ", %" #sb " offset:3072\n\t"
template <int P> DEVI void dma8(const void* sbase, const unsigned* v, unsigned lb) {
  asm volatile(DMA4(0, 1, 2, 3, 8, 9, 0, 10, 11) DMA4(4, 5, 6, 7, 8, 9, 4096, 10, 11)
               :: "v"(v[0]), "v"(v[1]), "v"(v[2]), "v"(v[3]), "v"(v[4]), "v"(v[5]), "v"(v[6]), "v"(v[7]), "s"(sbase), "s"(lb), "i"(P), "i"(P ? CAP : 63) : "memory", "scc");
}
template <int P> DEVI void dma12(const void* sbase, const unsigned* v, unsigned lb) {
  asm volatile(DMA4(0, 1, 2, 3, 12, 13, 0, 14, 15) DMA4(4, 5, 6, 7, 12, 13, 4096, 14, 15) DMA4(8, 9, 10, 11, 12, 13, 8192, 14, 15)
               :: "v"(v[0]), "v"(v[1]), "v"(v[2]), "v"(v[3]), "v"(v[4]), "v"(v[5]), "v"(v[6]), "v"(v[7]), "v"(v[8]), "v"(v[9]), "v"(v[10]), "v"(v[11]),
                  "s"(sbase), "s"(lb), "i"(P), "i"(P ? CAP : 63) : "memory", "scc");
}
DEVI void touch(const void* sbase, unsigned voff, unsigned ldsdump) {
  asm volatile("s_mov_b32 m0, %2\n\ts_nop 0\n\tglobal_load_lds_dword %0, %1" :: "v"(voff), "s"(sbase), "s"(ldsdump) : "memory");
}
#define CMM(r, a, x) "v_pk_mul_f32 %" #r ", %" #a ", %" #x " op_sel:[1,1] op_sel_hi:[1,0]\n\t"
#define CMF(r, a, x) "v_pk_fma_f32 %" #r ", %" #a ", %" #x ", %" #r " op_sel_hi:[0,1,1] neg_lo:[0,0,1]\n\t"
DEVI void cm4(f32x2& r0, f32x2& r1, f32x2& r2, f32x2& r3, f32x2 a0, f32x2 x0, f32x2 a1, f32x2 x1, f32x2 a2, f32x2 x2, f32x2 a3, f32x2 x3) {
  asm(CMM(0, 4, 5) CMM(1, 6, 7) CMM(2, 8, 9) CMM(3, 10, 11) CMF(0, 4, 5) CMF(1, 6, 7) CMF(2, 8, 9) CMF(3, 10, 11)
      : "=&v"(r0), "=&v"(r1), "=&v"(r2), "=&v"(r3) : "v"(a0), "v"(x0), "v"(a1), "v"(x1), "v"(a2), "v"(x2), "v"(a3), "v"(x3));
}
DEVI void put1(unsigned addr, unsigned v) {
  asm volatile("s_mov_b64 exec, 1\n\tds_write_b32 %0, %1\n\ts_mov_b64 exec, -1" :: "v"(addr), "v"(v) : "memory");
}
DEVI void prog_set(LAS char* L, int w, unsigned v) { put1((unsigned)(size_t)L + L_PROG + w * 4, v); }
DEVI void prog_done(LAS char* L, int w, unsigned v) { waitl(); prog_set(L, w, v); }
DEVI unsigned paddr(unsigned lbase, int lane, int w0, int mask) { return lbase + L_PROG + (w0 + (lane & mask)) * 4; }
DEVI void spin(unsigned addr, unsigned n) {
  unsigned t, c = 1u << 22;
  asm volatile("1:\n\tds_read_b32 %0, %2\n\ts_sub_u32 %1, %1, 1\n\ts_waitcnt lgkmcnt(0)\n\tv_cmp_gt_u32 vcc, %3, %0\n\ts_cselect_b64 vcc, 0, vcc\n\ts_cbranch_vccnz 1b"
               : "=&v"(t), "+s"(c) : "v"(addr), "s"(n) : "memory", "vcc", "scc");
}
DEVI void spin_if(unsigned seen, unsigned addr, unsigned n) {
  unsigned t, c;
  asm volatile("s_cmp_ge_u32 %4, %3\n\ts_cbranch_scc1 2f\n\ts_mov_b32 %1, 0x400000\n"
               "1:\n\tds_read_b32 %0, %2\n\ts_sub_u32 %1, %1, 1\n\ts_waitcnt lgkmcnt(0)\n\tv_cmp_gt_u32 vcc, %3, %0\n\ts_cselect_b64 vcc, 0, vcc\n\ts_cbranch_vccnz 1b\n2:"
               : "=&v"(t), "=&s"(c) : "v"(addr), "s"(n), "s"(seen) : "memory", "vcc", "scc");
}
DEVI void spin_slow(unsigned addr, unsigned n) {
  unsigned t, c = 1u << 21;
  asm volatile("1:\n\tds_read_b32 %0, %2\n\ts_waitcnt lgkmcnt(0)\n\tv_cmp_gt_u32 vcc, %3, %0\n\ts_cbranch_vccz 2f\n\ts_sleep 2\n\ts_sub_u32 %1, %1, 1\n\ts_cbranch_scc0 1b\n2:"
               : "=&v"(t), "+s"(c) : "v"(addr), "s"(n) : "memory", "vcc", "scc");
}
DEVI void poll_eq(const void* sbase, unsigned voff, unsigned want) {
  unsigned t, c = SPIN_CAP;
  asm volatile("1:\n\tglobal_load_dword %0, %2, %3 sc1\n\ts_sub_u32 %1, %1, 1\n\ts_waitcnt vmcnt(0)\n\tv_cmp_ne_u32 vcc, %4, %0\n\ts_cselect_b64 vcc, 0, vcc\n\ts_cbranch_vccnz 1b"
               : "=&v"(t), "+s"(c) : "v"(voff), "s"(sbase), "s"(want) : "memory", "vcc", "scc");
}
DEVI unsigned peek(unsigned addr) { return *(volatile LAS unsigned*)(size_t)addr; }
template <int N> struct IC { static constexpr int value = N; };
struct Args {
  const u16 *x, *are, *aim, *bre, *bim, *cre, *cim, *logdt, *dsk, *wg, *wu, *wd;
  u16* out;
  char* ws;
};

__global__ void __launch_bounds__(512, 2) fused_kernel(Args a) {
  extern __shared__ __attribute__((aligned(16))) char smem[];
  LAS char* L = (LAS char*)smem;
  const unsigned lbase = (unsigned)(size_t)L;
  unsigned ka_touch;
  asm volatile("s_load_dword %0, %1, 0x0" : "=s"(ka_touch) : "s"(__builtin_amdgcn_kernarg_segment_ptr()));
  unsigned long long pc; unsigned sz;
  asm volatile("s_getpc_b64 %0\n.Lfk_start:\n\ts_mov_b32 %1, .Lfk_end-.Lfk_start\n\ts_max_u32 %1, %1, .Lfk_e1-.Lfk_start\n\ts_max_u32 %1, %1, .Lfk_e2-.Lfk_start\n\ts_max_u32 %1, %1, .Lfk_e3-.Lfk_start"
               : "=s"(pc), "=s"(sz) :: "scc");
  const int tid = threadIdx.x, lane = tid & 63;
  const int w = __builtin_amdgcn_readfirstlane(tid >> 6);
  const int hw = w & 3;
  const int b = blockIdx.x, xcd = b & 7, slot = b >> 3;
  unsigned codetouch = 0;
  {
    const unsigned off = (unsigned)((lane * 8 + (slot & 7)) * 128);
    if (w == 3 && off < sz) asm volatile("global_load_dword %0, %1, %2" : "=v"(codetouch) : "v"(off), "s"(pc) : "memory");
  }
  prog_set(L, w, 0u); prog_set(L, w + 8, 0u);
  const int tl = 30 * (xcd & 3) + slot;
  const int m = (xcd >> 2) * 8 + (tl & 7), j = slot < 30 ? tl >> 3 : 15;
  const int jl = (xcd & 3) * 4 + (slot & 3);
  const int f0 = min(96 * j, FF - 96);
  const int m2 = xcd * 4 + (slot >> 3), n2 = slot & 7;
  const int t0 = ((xcd >> 2) * 8 + (slot >> 2)) * 128 + (jl >> 1) * 16;
  const int i16 = lane & 15, g = lane >> 4;
  char* ws = a.ws;
  const int rot2 = ((n2 * 11) / 4 + ((slot >> 3) * 11) / 2 + xcd * 3) % 22;
  const unsigned vd0 = (lane >> 3) * 1024 + (((lane & 7) ^ ((g & 1) << 2)) * 16);
  const unsigned pa_c = paddr(lbase, lane, 0, 3);

  if (w < 4) {
    unsigned pq[7], epoch_v;
    {
      const unsigned po = (unsigned)(b * 256 + tid) * 2;
      ld2(pq[6], a.logdt, (unsigned)(2 * b + (w >> 1)) * 2);
      ld2(pq[0], a.are, po); ld2(pq[1], a.aim, po); ld2(pq[2], a.bre, po); ld2(pq[3], a.bim, po); ld2(pq[4], a.cre, po); ld2(pq[5], a.cim, po);
      ld4c(epoch_v, ws + WS_EPOCH, 0u);
    }
    bar();
    const int tg = w & 1, fh = w >> 1;
    const int r = lane & 31, h = lane >> 5;
    const unsigned one2 = ((i16 & 7) == 4 * (g & 1)) ? 0x3F803F80u : 0u;
    v4u sel = {one2, one2, one2, one2};
    int xo[4], wo[2];
#pragma unroll
    for (int p = 0; p < 4; ++p) xo[p] = (tg * 64 + r) * 128 + (((2 * (p ^ (2 * tg)) + h) ^ ((r >> 1) & 7)) * 16);
    wo[0] = WGO + (g & 1) * 12320 + (8 * h + (i16 >> 2)) * 192 + fh * 96 + (lane & 3) * 8;
    wo[1] = wo[0] + (1 - tg) * 6144;
    wo[0] += tg * 6144;
    int zrow;
    {
      const int e = (r & 3) + 4 * (r >> 3), hh = (r >> 2) & 1;
      const unsigned long long lo = hh ? 0x1019181818170909ull : 0x0706050403020100ull;
      const unsigned long long hi = hh ? 0x160F150E121B0B10ull : 0x140D130C111A0A08ull;
      zrow = (int)(((e < 8 ? lo : hi) >> (8 * (e & 7))) & 31);
    }
    int zc_ = L_ZT + w * ZTS + zrow * 272 + h * 16, zf_ = L_ZT + w * ZTS + r * 272 + h * 16;
    const unsigned pa_l = lbase + L_PROG + 16;
#pragma unroll
    for (int p = 0; p < 4; ++p) PIN(xo[p]);
    PIN(wo[0]); PIN(wo[1]); PIN(zc_); PIN(zf_); PIN(sel);
    waitv<0>();
#pragma unroll
    for (int e = 0; e < 7; ++e) PIN(pq[e]);
    PIN(codetouch); PIN(epoch_v);
    const unsigned tag = __builtin_amdgcn_readfirstlane(epoch_v) * 0x9E3779B1u + 0x7F4A7C15u;
    {
      const float dt = rbf(expf(bf2f(pq[6])));
      const float ar = (dt * bf2f(pq[0])) * 1.44269504088896341f;
      float ai = (dt * bf2f(pq[1])) * 0.15915494309189535f;
      ai -= __builtin_rintf(ai);
      const float br = dt * bf2f(pq[2]), bi = dt * bf2f(pq[3]), cr0 = bf2f(pq[4]), ci0 = bf2f(pq[5]);
      f32x2 C = {cr0 * br - ci0 * bi, -(cr0 * bi + ci0 * br)};
      const f32x2 zc = f32x2{__builtin_amdgcn_cosf(ai), -__builtin_amdgcn_sinf(ai)} * __builtin_amdgcn_exp2f(ar);
      const f32x2 u8 = f32x2{__builtin_amdgcn_cosf(ai * 8.f), __builtin_amdgcn_sinf(ai * 8.f)} * __builtin_amdgcn_exp2f(ar * 8.f);
      f32x2 b0 = f32x2{__builtin_amdgcn_cosf(ai * 128.f), __builtin_amdgcn_sinf(ai * 128.f)} * __builtin_amdgcn_exp2f(ar * 128.f);
      const f32x2 u8c = {u8.x, -u8.y}, b0c = {b0.x, -b0.y};
      LAS unsigned* zt = (LAS unsigned*)(L + L_ZT + w * ZTS + lane * 4);
      f32x2 vc, E, b1, u16_, D = C, ex = C;
      cm4(vc, E, b1, u16_, b0c, b0c, C, u8c, b0, u8, u8, u8);
      zt[0] = pk2v(C); zt[8 * 68] = pk2v(E);
      f32x2 c6 = C;
#pragma unroll
      for (int k = 1; k < 8; ++k) {
        if (k == 3 || k == 5) ex = C;
        if (k == 7) c6 = C;
        cm4(C, D, E, ex, C, zc, D, vc, E, vc, ex, vc);
        zt[k * 68] = pk2v(C); zt[(8 + k) * 68] = pk2v(D); zt[(15 + k) * 68] = pk2v(E);
        if (k == 3) zt[23 * 68] = pk2v(ex);
        if (k == 5) zt[24 * 68] = pk2v(ex);
        if (k == 6) zt[26 * 68] = pk2v(ex);
        if (k == 7) zt[27 * 68] = pk2v(ex);
      }
      f32x2 a0 = u16_, a1, b2, b3;
      cm4(c6, a1, b2, b3, c6, vc, u8, u16_, b0, u16_, b1, u16_);
      zt[25 * 68] = pk2v(c6);
      asm volatile("" ::: "memory");
      v4u fc[8], ff[8];
#pragma unroll
      for (int ks = 0; ks < 8; ++ks) fc[ks] = *(LAS v4u*)(L + zc_ + ks * 32);
      asm volatile("" ::: "memory");
      zt[0] = 0x00003F80u; zt[68] = pk2v(u8); zt[16 * 68] = pk2v(b0); zt[17 * 68] = pk2v(b1);
      zt[2 * 68] = pk2v(a0); zt[3 * 68] = pk2v(a1); zt[18 * 68] = pk2v(b2); zt[19 * 68] = pk2v(b3);
#pragma unroll
      for (int k = 2; k < 8; ++k) {
        cm4(a0, a1, b2, b3, a0, u16_, a1, u16_, b2, u16_, b3, u16_);
        zt[2 * k * 68] = pk2v(a0); zt[(2 * k + 1) * 68] = pk2v(a1); zt[(16 + 2 * k) * 68] = pk2v(b2); zt[(17 + 2 * k) * 68] = pk2v(b3);
      }
      asm volatile("" ::: "memory");
#pragma unroll
      for (int ks = 0; ks < 8; ++ks) ff[ks] = *(LAS v4u*)(L + zf_ + ks * 32);
      sched0();
      f32x16 kd = {};
#pragma unroll
      for (int ks = 0; ks < 8; ++ks) kd = mfma32(fc[ks], ff[ks], kd);
#pragma unroll
      for (int g4 = 0; g4 < 4; ++g4) *(LAS f32x4*)(L + part_off(w) + g4 * 1024 + lane * 16) = f32x4{kd[4 * g4], kd[4 * g4 + 1], kd[4 * g4 + 2], kd[4 * g4 + 3]};
      asm volatile("" ::: "memory");
      prog_set(L, w, 1u);
    }
    if (2 * j - fh < 28) {
      f32x16 acc[2][3];
#pragma unroll
      for (int i = 0; i < 2; ++i)
#pragma unroll
        for (int c = 0; c < 3; ++c) acc[i][c] = f32x16{};
      f32x4 cs[3] = {};
      v4u xf[2][2], wf[2][3];
      auto ldf = [&](int buf, int sl, int p) {
        LAS char* S = L + sl * RS;
        LAS char* Wp = S + wo[p >> 1] + (p & 1) * 3072;
        wf[buf][0] = trd2(Wp, Wp + 768);
        xf[buf][0] = *(LAS v4u*)(S + xo[p]);
        xf[buf][1] = *(LAS v4u*)(S + xo[p] + 4096);
        wf[buf][1] = trd2(Wp + 32, Wp + 800);
        wf[buf][2] = trd2(Wp + 64, Wp + 832);
      };
      auto mmc = [&](int buf, int c) { acc[0][c] = mfma32(wf[buf][c], xf[buf][0], acc[0][c]); acc[1][c] = mfma32(wf[buf][c], xf[buf][1], acc[1][c]); };
      auto mm = [&](int buf, bool dosel) {
        mmc(buf, 0); mmc(buf, 1); mmc(buf, 2);
        if (dosel) {
#pragma unroll
          for (int c = 0; c < 3; ++c) cs[c] = mfma16(sel, wf[buf][c], cs[c]);
        }
      };
      auto pattern = [&]() { SGB(0x8, 1); SGB(0x100, 2); SGB(0x8, 1); SGB(0x100, 2); SGB(0x8, 1); SGB(0x100, 2); SGB(0x8, 1); SGB(0x100, 3); SGB(0x8, 5); sched0(); };
      spin(pa_l, 1u);
      ldf(0, 0, 0);
      sched0();
      unsigned pw = lbase + L_PROG + w * 4;
      unsigned pl[2] = {pa_l, pa_l + 4};
      PIN(pw); PIN(pl[0]); PIN(pl[1]);
#pragma unroll
      for (int s = 0; s < 7; ++s) {
        const int sl = s % 3;
        ldf(1, sl, 1); mm(0, true); pattern();
        ldf(0, sl, 2);
        const unsigned full = peek(pl[(s + 1) & 1]);
        unsigned pv = s + 2;
        PIN(pv);
        mm(1, true); pattern();
        ldf(1, sl, 3);
        unsigned fs = __builtin_amdgcn_readfirstlane(full);
        mmc(0, 0); mmc(0, 1);
        SGB(0x8, 1); SGB(0x100, 2); SGB(0x2, 1); SGB(0x8, 1); SGB(0x100, 2); SGB(0x8, 1); SGB(0x100, 2); SGB(0x8, 1); SGB(0x100, 2);
        sched0();
        if (s == 6) {
#pragma unroll
          for (int c = 0; c < 3; ++c) *(LAS float*)(L + L_CSUM + w * 768 + c * 256 + lane * 4) = cs[c][0];
          asm volatile("" ::: "memory");
        }
        put1(pw, pv);
        sched0();
        mmc(0, 2);
        sched0();
        spin_if(fs, pl[(s + 1) & 1], s + 2);
        asm volatile("" ::: "memory");
        sched0();
        ldf(0, (s + 1) % 3, 0); mm(1, false); pattern();
      }
      const unsigned pq_ = peek(lbase + L_PROG + 24);
      ldf(1, 1, 1);
      const unsigned pp_ = peek(lbase + L_PROG + (w ^ 1) * 4);
      mmc(0, 0); mmc(0, 1);
      SGB(0x8, 1); SGB(0x100, 3); SGB(0x8, 1); SGB(0x100, 3); SGB(0x8, 1); SGB(0x100, 2); SGB(0x8, 1); SGB(0x100, 2); sched0();
      const unsigned ws_ = __builtin_amdgcn_readfirstlane(pq_);
      mmc(0, 2);
#pragma unroll
      for (int c = 0; c < 3; ++c) cs[c] = mfma16(sel, wf[0][c], cs[c]);
      sched0();
      spin_if(ws_, lbase + L_PROG + 24, 3u);
      sched0();
      const unsigned sv[2] = {*(LAS unsigned*)(L + L_STAT + (tg * 64 + r) * 4), *(LAS unsigned*)(L + L_STAT + (tg * 64 + 32 + r) * 4)};
      ldf(0, 1, 2);
      const unsigned ps_ = __builtin_amdgcn_readfirstlane(pp_);
      float c1[2], ir2[2];
      v4u xa[2], wa[3];
#pragma unroll
      for (int i = 0; i < 2; ++i) {
        const unsigned nm = h ? 0u : (sv[i] & 0xffffu) ^ 0x8000u;
        xa[i] = v4u{nm | (nm << 16), nm, 0u, 0u};
        const float sd = bfhi(sv[i]);
        c1[i] = cut(-1.44269504088896341f * __builtin_amdgcn_rcpf(sd)); ir2[i] = cut(sd * sd);
        asm("" : "+v"(xa[i]));
      }
      mm(1, true);
      SGB(0x8, 1); SGB(0x100, 3); SGB(0x2, 2); SGB(0x8, 1); SGB(0x100, 3); SGB(0x8, 1); SGB(0x100, 2); SGB(0x8, 1); SGB(0x100, 2); SGB(0x2, 3);
      SGB(0x8, 1); SGB(0x2, 5); SGB(0x8, 1); SGB(0x2, 5); SGB(0x8, 1); SGB(0x2, 2); SGB(0x8, 1); SGB(0x2, 2); SGB(0x8, 1); sched0();
      spin_if(ps_, lbase + L_PROG + (w ^ 1) * 4, 8u);
      asm volatile("" ::: "memory");
      sched0();
      ldf(1, 1, 3);
      float csp[3];
#pragma unroll
      for (int c = 0; c < 3; ++c) csp[c] = *(LAS float*)(L + L_CSUM + (w ^ 1) * 768 + c * 256 + lane * 4);
      mmc(0, 0); mmc(0, 1);
      SGB(0x8, 1); SGB(0x100, 3); SGB(0x8, 1); SGB(0x100, 3); SGB(0x8, 1); SGB(0x100, 3); SGB(0x8, 1); SGB(0x100, 2); sched0();
      __builtin_amdgcn_s_setprio(1);
      put1(pw, 10u);
      sched0();
      mmc(0, 2);
#pragma unroll
      for (int c = 0; c < 3; ++c) cs[c] = mfma16(sel, wf[0][c], cs[c]);
#pragma unroll
      for (int c = 0; c < 3; ++c) cs[c] = mfma16(sel, wf[1][c], cs[c]);
      sched0();
      mmc(1, 0);
      sched0();
#pragma unroll
      for (int c = 0; c < 3; ++c) {
        const float s = cut(cs[c][0] + csp[c]);
        const float hi = bfhi(__builtin_bit_cast(unsigned, s)), d1 = cut(s - hi), mid = bfhi(__builtin_bit_cast(unsigned, d1)), lo = cut(d1 - mid);
        wa[c] = v4u{__builtin_amdgcn_perm(__builtin_bit_cast(unsigned, d1), __builtin_bit_cast(unsigned, s), 0x07060302u), __builtin_bit_cast(unsigned, lo) >> 16, 0u, 0u};
      }
      mmc(1, 1); mmc(1, 2);
      SGB(0x2, 5); SGB(0x8, 1); SGB(0x2, 6); SGB(0x8, 1); SGB(0x2, 6); SGB(0x8, 1); SGB(0x2, 6); SGB(0x8, 1); sched0();
#pragma unroll
      for (int c = 0; c < 3; ++c) { acc[0][c] = mfma32(wa[c], xa[0], acc[0][c]); acc[1][c] = mfma32(wa[c], xa[1], acc[1][c]); }
      const __amdgpu_buffer_rsrc_t rhs = __builtin_amdgcn_make_buffer_rsrc((void*)(ws + WS_H), 0, H_BYTES, 0x00020000);
#pragma unroll
      for (int c = 0; c < 3; ++c) {
        const int F16 = f0 / 16 + fh * 3 + c;
#pragma unroll
        for (int i = 0; i < 2; ++i) {
          float hv[8];
#pragma unroll
          for (int e = 0; e < 8; ++e) {
            const float gv = acc[i][c][e], u = acc[i][c][e + 8];
            const float ex = __builtin_amdgcn_exp2f(gv * c1[i]);
            hv[e] = (gv * u) * __builtin_amdgcn_rcpf(ex * ir2[i] + ir2[i]);
          }
          const v4u o = {pk2(hv[0], hv[1]), pk2(hv[2], hv[3]), pk2(hv[4], hv[5]), pk2(hv[6], hv[7])};
          const int so = (((m * 4 + tg * 2 + i) * 86 + F16) * 1024) | (((F16 - 6 * j) >> 31) & 0x40000000);
          __builtin_amdgcn_raw_buffer_store_b128(o, rhs, lane * 16, so, 16);
          asm volatile("s_nop 1");
          sched0();
        }
      }
    }
    prog_set(L, w, 12u);
    const int cl = w >> 1, rh_ = w & 1;
    f32x4 y[8];
#pragma unroll
    for (int t = 0; t < 8; ++t) y[t] = f32x4{};
    {
      const int s0 = 2031 - i16 + 8 * g - rh_ * 128 - 96;
      LAS char* ka = L + L_KIMG + cl * 2 * CS + (s0 & 1) * CS + ((s0 + 1) >> 1) * 4;
      LAS char* ub = L + L_UIMG + cl * US + 512 + (16 * i16 - 16 + 8 * g - rh_ * 128 - 96) * 2;
      LAS char* kq_[4];
#pragma unroll
      for (int q = 0; q < 4; ++q) { kq_[q] = ka - (2 * q + 1) * 512; asm volatile("" : "+v"(kq_[q])); }
      v4u fa[2][8], fb[2][8];
      auto ldc = [&](int buf, int rr_) {
#pragma unroll
        for (int q = 0; q < 8; ++q) {
          LAS unsigned* p = (LAS unsigned*)(kq_[q >> 1] + ((q & 1) ? 0 : 512) + (3 - rr_) * 64);
          fa[buf][q] = v4u{p[0], p[1], p[2], p[3]};
          fb[buf][q] = *(LAS v4u*)(ub + q * 512 + (3 - rr_) * 64);
        }
      };
      auto publish = [&]() {
        waitv<0>();
        if (j < 15) flag1(ws + WS_F2, (unsigned)(((m * 2 + tg) * 16 + j) * 2 + fh) * 128, tag);
        prog_set(L, w, 13u);
      };
      const unsigned pa_w = paddr(lbase, lane, 6, 1);
      bool early = true;
      if (__any(peek(pa_w) < 5u)) { publish(); early = false; spin_slow(pa_w, 5u); }
      ldc(0, 0);
      sched0();
      if (early) publish();
      sched0();
#pragma unroll
      for (int rr_ = 0; rr_ < 4; ++rr_) {
        if (rr_ < 3) ldc((rr_ + 1) & 1, rr_ + 1);
#pragma unroll
        for (int d = 0; d < 8; ++d)
#pragma unroll
          for (int o = 0; o <= d; ++o) {
            if (d + o < 8) y[d + o] = mfma16(fa[rr_ & 1][d], fb[rr_ & 1][o], y[d + o]);
            if (o < d && d + o < 8) y[d + o] = mfma16(fa[rr_ & 1][o], fb[rr_ & 1][d], y[d + o]);
          }
        if (rr_ < 3) {
#pragma unroll
          for (int k = 0; k < 24; ++k) { SGB(0x8, 1); SGB(0x100, 1); }
        }
        sched0();
        if (rr_ == 2) { asm volatile("" ::: "memory"); prog_set(L, w, 20u); }
        sched0();
      }
    }
    LAS char* xo_ = L + L_KIMG + cl * 2 * CS + rh_ * 4096;
    LAS char* xi_ = L + L_KIMG + cl * 2 * CS + (rh_ ^ 1) * 4096;
    const unsigned pa_p = lbase + L_PROG + (w ^ 1) * 4;
    const unsigned yo = (unsigned)((((rh_ * 16 + (i16 >> 2)) * 8 + (b >> 5)) * 64 + ((2 * b + cl) & 63)) * 256 + g * 64 + (i16 & 3) * 16);
    auto finish = [&](auto keep) {
      constexpr int K = decltype(keep)::value;
      spin(pa_p, 20u);
#pragma unroll
      for (int t = 0; t < 4; ++t) *(LAS f32x4*)(xo_ + t * 1024 + lane * 16) = y[(K ^ 4) + t];
      asm volatile("" ::: "memory");
      prog_set(L, w, 21u);
      spin(pa_p, 21u);
      f32x4 yp[4];
#pragma unroll
      for (int t = 0; t < 4; ++t) yp[t] = *(LAS f32x4*)(xi_ + t * 1024 + lane * 16);
#pragma unroll
      for (int t = 0; t < 4; ++t) {
        const f32x4 o = yp[t];
        const v4u v = {pk2s(y[K + t][0] + o[0], y[K + t][1] + o[1]), tag, pk2s(y[K + t][2] + o[2], y[K + t][3] + o[3]), tag};
        st16c(ws + WS_YT + t * (4 * 8 * 64 * 256), yo, v);
      }
    };
    if (rh_) finish(IC<4>{}); else finish(IC<0>{});
    __builtin_amdgcn_s_setprio(0);
    spin_slow(lbase + L_PROG + (4 + w) * 4, 14u);
    v4u a0, a1, a2, a3;
    {
      const char* yb = ws + WS_YT + (m2 * 8 + n2) * 16384 + hw * 4096;
#pragma unroll 1
      for (unsigned sp = 0; sp < SPIN_CAP; ++sp) {
        ld16c(a0, yb, lane * 16); ld16c(a1, yb + 1024, lane * 16); ld16c(a2, yb + 2048, lane * 16); ld16c(a3, yb + 3072, lane * 16);
        waitv<0>(); PIN(a0); PIN(a1); PIN(a2); PIN(a3);
        if (__all(a0.y == tag && a0.w == tag && a1.y == tag && a1.w == tag && a2.y == tag && a2.w == tag && a3.y == tag && a3.w == tag)) break;
        __builtin_amdgcn_s_sleep(6);
      }
    }
    {
      LAS char* p = xi_ + g * 128 + ((i16 & 3) * 16 + (i16 >> 2) * 4) * 2;
      *(LAS v2u*)p = v2u{a0.x, a0.z}; *(LAS v2u*)(p + 512) = v2u{a1.x, a1.z}; *(LAS v2u*)(p + 1024) = v2u{a2.x, a2.z}; *(LAS v2u*)(p + 1536) = v2u{a3.x, a3.z};
    }
    {
      v4u av[2], xv[2];
#pragma unroll
      for (int it = 0; it < 2; ++it) {
        const int c8 = hw * 2 + it;
        LAS char* p = xi_ + (it * 8 + (i16 >> 2)) * 128 + (g * 16 + (i16 & 3) * 4) * 2;
        av[it] = trd2(p, p + 512);
        xv[it] = *(LAS v4u*)(L + L_BASE + lane * 128 + ((c8 ^ (lane & 7)) * 16));
      }
#pragma unroll
      for (int it = 0; it < 2; ++it) {
        v4u o;
#pragma unroll
        for (int e = 0; e < 4; ++e) o[e] = pk2s(bflo(xv[it][e]) + bflo(av[it][e]), bfhi(xv[it][e]) + bfhi(av[it][e]));
        *(LAS v4u*)(L + L_BASE + lane * 128 + (((hw * 2 + it) ^ (lane & 7)) * 16)) = o;
      }
    }
    asm volatile(".Lfk_e2:");
  } else {
    const unsigned pa_w = paddr(lbase, lane, 6, 1);
    const int kq = hw, r = lane & 31, h = lane >> 5;
    LAS char* W = L + kq * 2048;
    const int fb = min(22 * kq, 64);
    const int wa0 = (4 * h + (i16 >> 2)) * 128 + (i16 >> 3) * 64 + (g & 1) * 32 + (lane & 3) * 8;
    const int wa1 = wa0 ^ 64;
    const char* wds = (const char*)a.wd + (size_t)fb * 16384 + n2 * 128;
    int wi = rot2 + 5;
    wi = wi >= 22 ? wi - 22 : wi;
    auto wd_steps = [&](int n, int pos) {
#pragma unroll 1
      for (int i = 0; i < n; ++i) {
        dma2(wds + (size_t)wi * 16384, vd0, vd0 + 7168, lbase + pos + kq * 2048);
        wi = wi == 21 ? 0 : wi + 1;
        pos += 8192;
        waitv<WWIN>();
      }
    };
    if (w >= 6) {
      const int wk = w - 6, wt = tid - 384;
      v4u rr[8];
      unsigned epoch_w, dsk_v;
      bar();
      {
        const char* own = (const char*)a.x + (size_t)(t0 + 4 * wk) * 1024 + (jl & 1) * 512;
        const char* oth = (const char*)a.x + (size_t)(t0 + 4 * wk) * 1024 + 512 - (jl & 1) * 512;
        const unsigned vo = g * 1024 + i16 * 16, vo2 = vo + 256, vo3 = vo + 8192, vo4 = vo + 8448;
        ld16(rr[0], own, vo); ld16(rr[1], own, vo2); ld16(rr[2], oth, vo); ld16(rr[3], oth, vo2);
        ld16(rr[4], own, vo3); ld16(rr[5], own, vo4); ld16(rr[6], oth, vo3); ld16(rr[7], oth, vo4);
        ld4c(epoch_w, ws + WS_EPOCH, 0u);
        ld2(dsk_v, a.dsk, (unsigned)(2 * b + wk) * 2);
      }
      waitv<0>();
#pragma unroll
      for (int e = 0; e < 8; ++e) PIN(rr[e]);
      PIN(epoch_w); PIN(dsk_v);
      const unsigned epoch = __builtin_amdgcn_readfirstlane(epoch_w);
      const unsigned tag = epoch * 0x9E3779B1u + 0x7F4A7C15u;
      prog_set(L, 8, tag);
      v4u on[4];
      {
        float inv[2], nb[2];
#pragma unroll
        for (int q = 0; q < 2; ++q) {
          float s0 = 0.f, s1 = 0.f, q0 = 0.f, q1 = 0.f;
#pragma unroll
          for (int i = 0; i < 4; ++i) {
            s0 = dot2(rr[4 * q][i], 0x3F803F80u, s0); q0 = dot2(rr[4 * q][i], rr[4 * q][i], q0);
            s1 = dot2(rr[4 * q + 1][i], 0x3F803F80u, s1); q1 = dot2(rr[4 * q + 1][i], rr[4 * q + 1][i], q1);
            s0 = dot2(rr[4 * q + 2][i], 0x3F803F80u, s0); q0 = dot2(rr[4 * q + 2][i], rr[4 * q + 2][i], q0);
            s1 = dot2(rr[4 * q + 3][i], 0x3F803F80u, s1); q1 = dot2(rr[4 * q + 3][i], rr[4 * q + 3][i], q1);
          }
          const float mean = rsum16(cut(s0 + s1)) * (1.f / DM);
          const float var = fmaxf(rsum16(cut(q0 + q1)) * (1.f / DM) - mean * mean, 0.f);
          const float mu = rbf(mean), sd = rbf(__builtin_amdgcn_sqrtf(rbf(rbf(var) + 1e-5f)));
          inv[q] = __builtin_amdgcn_rcpf(sd); nb[q] = -mu * inv[q];
          if (i16 == 0 && !(jl & 1)) st8c(ws + WS_STAT, (unsigned)(t0 + 4 * wk + g + 8 * q) * 8, v2u{pk2(mu, sd), tag});
        }
#pragma unroll
        for (int k = 0; k < 2; ++k) {
          unsigned o[2][4];
#pragma unroll
          for (int q = 0; q < 2; ++q)
#pragma unroll
            for (int i = 0; i < 4; ++i) o[q][i] = pk2s(bflo(rr[4 * q + k][i]) * inv[q] + nb[q], bfhi(rr[4 * q + k][i]) * inv[q] + nb[q]);
          on[2 * k] = v4u{o[0][0], o[1][0], o[0][1], o[1][1]};
          on[2 * k + 1] = v4u{o[0][2], o[1][2], o[0][3], o[1][3]};
          PIN(on[2 * k]); PIN(on[2 * k + 1]);
        }
      }
      spin_slow(pa_c, 1u);
      f32x4 pt[2][4];
      {
        LAS char* p = L + L_UIMG + (4 * wk + g) * LNS + i16 * 32;
        *(LAS v4u*)p = on[0]; *(LAS v4u*)(p + 16) = on[1]; *(LAS v4u*)(p + 512) = on[2]; *(LAS v4u*)(p + 528) = on[3];
#pragma unroll
        for (int g4 = 0; g4 < 4; ++g4) { pt[0][g4] = *(LAS f32x4*)(L + part_off(2 * wk) + g4 * 1024 + lane * 16); pt[1][g4] = *(LAS f32x4*)(L + part_off(2 * wk + 1) + g4 * 1024 + lane * 16); }
      }
      asm volatile("" ::: "memory");
      prog_set(L, w, 1u);
      spin(pa_w, 1u);
      {
        LAS char* xp = L + L_UIMG + (i16 >> 2) * LNS + (wk * 64 + g * 4 + (lane & 3)) * 8;
        const unsigned xto = (unsigned)(((jl & 1) * 128 + wk * 64 + g * 4 + (i16 >> 2)) * 8192 + (t0 >> 4) * 64 + (lane & 3) * 16);
        v4u xv[4];
#pragma unroll
        for (int it = 0; it < 4; ++it) xv[it] = trd2(xp + it * 128, xp + it * 128 + 4 * LNS);
#pragma unroll
        for (int it = 0; it < 4; ++it) st16c(ws + WS_XT + it * 16 * 8192, xto, xv[it]);
      }
      {
        float v[16];
#pragma unroll
        for (int e = 0; e < 16; ++e) v[e] = cut(pt[0][e >> 2][e & 3] + pt[1][e >> 2][e & 3]);
        if (lane == 0) v[0] += bf2f(dsk_v);
        const int B = lane & 31, hh = lane >> 5;
        LAS char* k0 = L + L_KIMG + wk * 2 * CS + (255 - B) * 16;
        LAS char* ka = k0 - hh * 512;
        *(LAS v4u*)ka = v4u{pk2(v[7], v[6]), pk2(v[5], v[4]), pk2(v[3], v[2]), pk2(v[1], v[0])};
        *(LAS v4u*)(ka + CS) = v4u{pk2(v[8], v[7]), pk2(v[6], v[5]), pk2(v[4], v[3]), pk2(v[2], v[1])};
        const unsigned p00 = pk2(v[9], v[9]), p44 = pk2(v[10], v[10]), p88 = pk2(v[11], v[11]);
        *(LAS v4u*)(ka - 1024) = v4u{p88, p44, pk2(v[10], v[9]), p00};
        *(LAS v4u*)(ka - 1024 + CS) = v4u{p88, pk2(v[11], v[10]), p44, p00};
        LAS char* kc = k0 - 2048 - hh * 1024;
#pragma unroll
        for (int t = 0; t < 2; ++t) {
          const float lo = v[12 + 2 * t], hi = v[13 + 2 * t], md = (B & 1) ? hi : lo;
          const unsigned ll = pk2(lo, lo), hh2 = pk2(hi, hi);
          *(LAS v4u*)(kc - t * 512) = v4u{hh2, pk2(hi, md), ll, ll};
          *(LAS v4u*)(kc - t * 512 + CS) = v4u{hh2, hh2, pk2(md, lo), ll};
        }
        if (lane < 4) {
          *(LAS v4u*)(L + L_KIMG + wk * 2 * CS + 4096 + lane * 16) = v4u{0u, 0u, 0u, 0u};
          *(LAS v4u*)(L + L_KIMG + wk * 2 * CS + CS + 4096 + lane * 16) = v4u{lane == 0 ? pk2(v[0], 0.f) : 0u, 0u, 0u, 0u};
        }
      }
      waitv<0>();
      prog_done(L, w, 2u);
      if (__all(peek(pa_w) >= 2u)) { if (lane == 0) st4c(ws + WS_F1, (unsigned)b * 4, tag); }
      if (j == 15) { for (int i = 0; i < 6; ++i) __builtin_amdgcn_s_sleep(16); }
      else spin_slow(lbase + L_PROG + 16, 7u);
      v4u pfl, pst;
      ld16c(pst, ws + WS_STAT + m * 1024, lane * 16); ld16c(pfl, ws + WS_F1, lane * 16);
      waitv<0>(); PIN(pst); PIN(pfl);
      if (wk == 0) {
#pragma unroll 1
        for (unsigned sp = 0; sp < SPIN_CAP; ++sp) {
          if (__all(pst.y == tag && pst.w == tag)) break;
          __builtin_amdgcn_s_sleep(8);
          ld16c(pst, ws + WS_STAT + m * 1024, lane * 16); waitv<0>(); PIN(pst);
        }
        *(LAS v2u*)(L + L_STAT + lane * 8) = v2u{pst.x, pst.z};
        asm volatile("" ::: "memory");
        prog_set(L, w, 3u);
        asm volatile(".Lfk_e3:");
      }
#pragma unroll 1
      for (unsigned sp = 0; sp < SPIN_CAP; ++sp) {
        if (__all(pfl.x == tag && pfl.y == tag && pfl.z == tag && pfl.w == tag)) break;
        __builtin_amdgcn_s_sleep(8);
        ld16c(pfl, ws + WS_F1, lane * 16); waitv<0>(); PIN(pfl);
      }
      if (j < 15) spin(lbase + L_PROG + P_MARK, 2u);
      v4u u0, u1, u2, u3;
      const char* xb = ws + WS_XT + b * 8192;
      ld16c(u0, xb, wt * 16); ld16c(u1, xb + 2048, wt * 16); ld16c(u2, xb + 4096, wt * 16); ld16c(u3, xb + 6144, wt * 16);
      {
        const unsigned vo = (unsigned)((lane >> 3) * 1024 + (lane & 7) * 16);
        const char* xt = (const char*)a.x + (size_t)(m2 * 64 + wk * 32) * 1024 + n2 * 128;
        ld16(rr[0], xt, vo); ld16(rr[1], xt + 8192, vo); ld16(rr[2], xt + 16384, vo); ld16(rr[3], xt + 24576, vo);
      }
      if (b == 0 && wt == 0) st4c(ws + WS_EPOCH, 0u, epoch + 1u);
      LAS char* up = L + L_UIMG + (wt & 1) * US + 512 + ((wt >> 2) * 16 + ((wt >> 1) & 1) * 8) * 2;
      if (lane < 32) *(LAS v4u*)(L + L_UIMG + wk * US + lane * 16) = v4u{0u, 0u, 0u, 0u};
      waitv<0>(); PIN(u0); PIN(u1); PIN(u2); PIN(u3);
      *(LAS v4u*)up = u0; *(LAS v4u*)(up + 1024) = u1; *(LAS v4u*)(up + 2048) = u2; *(LAS v4u*)(up + 3072) = u3;
#pragma unroll
      for (int e = 0; e < 4; ++e) { PIN(rr[e]); *(LAS v4u*)(L + L_BASE + wk * 4096 + e * 1024 + (lane >> 3) * 128 + (((lane & 7) ^ (lane >> 3)) * 16)) = rr[e]; }
      asm volatile("" ::: "memory");
      prog_set(L, w, 5u);
      asm volatile(".Lfk_e1:");
    } else {
      __builtin_amdgcn_s_setprio(1);
      unsigned vx[8], vw[12];
      {
        const int r = lane >> 3, cp = lane & 7;
#pragma unroll
        for (int i = 0; i < 12; ++i) { const int n = (i % 3) * 64 + lane; vw[i] = ((i / 3) * 16 + n / 12) * (FF * 2) + (n % 12) * 16 - (i & 3) * 1024; PIN(vw[i]); }
#pragma unroll
        for (int i = 0; i < 8; ++i) { vx[i] = r * 1024 + ((cp ^ ((i & 1) * 4 + g)) * 16) + i * 8192 - (i & 3) * 1024; PIN(vx[i]); }
      }
      bar();
      const int rot1 = (m + 2 * j) & 7;
      const char* xs0 = (const char*)a.x + (size_t)(m * 128) * 1024;
      const char* gs0 = (const char*)a.wg + f0 * 2;
      const char* us0 = (const char*)a.wu + f0 * 2;
      auto stage = [&](auto pace, int s, int sl) {
        constexpr int P = decltype(pace)::value;
        const unsigned lb = lbase + sl * RS;
        const int ks = (s + rot1) & 7;
        dma12<P>(gs0 + (size_t)ks * (64 * FF * 2), vw, lb + WGO);
        dma8<P>(xs0 + ks * 128, vx, lb);
        dma12<P>(us0 + (size_t)ks * (64 * FF * 2), vw, lb + WUO);
        dma8<P>(xs0 + ks * 128 + 65536, vx, lb + 8192);
      };
      if (j < 15) {
        if (hw) spin(lbase + L_PROG + P_MARK, 1u);
        stage(IC<0>{}, hw, hw);
        if (!hw) put1(lbase + L_PROG + P_MARK, 1u);
        waitv<0>();
        prog_set(L, w, hw + 1);
        int sl = hw ? 0 : 2;
#pragma unroll 1
        for (int it = 1; it < 4; ++it) {
          const int s = 2 * it + hw;
          if (s >= 3) spin(pa_c, s - 1);
          stage(IC<PACE>{}, s, sl);
          sl = sl == 0 ? 2 : sl - 1;
          if (s == 7) put1(lbase + L_PROG + P_MARK, 2u);
          waitv<0>();
          prog_set(L, w, s + 1);
        }
      }
      if (!hw) {
        unsigned vd[8];
#pragma unroll
        for (int i = 0; i < 8; ++i) vd[i] = vd0 + (unsigned)(min(22 * (i >> 1), 64) * 16384 + (i & 1) * 8192 - (i & 3) * 1024);
        if (j < 15) { spin(lbase + L_PROG + P_MARK, 2u); __builtin_amdgcn_s_sleep(3); }
        spin(pa_c, 7u);
        int ii = rot2;
        {
          const int i6 = rot2 + 5 >= 22 ? rot2 - 17 : rot2 + 5;
          touch((const char*)a.wd + (size_t)i6 * 16384 + n2 * 128, (unsigned)(min(22 * g, 64) * 16 + i16) * 1024, lbase + L_CSUM + 3072);
        }
#pragma unroll 1
        for (int i = 0; i < 5; ++i) {
          ii = ii >= 22 ? ii - 22 : ii;
          dma8<0>((const char*)a.wd + (size_t)ii * 16384 + n2 * 128, vd, lbase + 2 * RS + i * 8192);
          ++ii;
        }
        waitv<0>();
        prog_set(L, w, 9u);
      }
      __builtin_amdgcn_s_setprio(0);
    }
    LAS char* wp[3][2];
#pragma unroll
    for (int k = 0; k < 3; ++k) { wp[k][0] = W + wd_pos(5 * k) + wa0; wp[k][1] = W + wd_pos(5 * k) + wa1; PIN(wp[k][0]); PIN(wp[k][1]); }
    auto wfrag = [&](v4u* d, int i) {
      d[0] = trd2(wp[i / 5][0] + (i % 5) * 8192, wp[i / 5][0] + (i % 5) * 8192 + 1024); d[1] = trd2(wp[i / 5][1] + (i % 5) * 8192, wp[i / 5][1] + (i % 5) * 8192 + 1024);
    };
    LAS char* pw_[8];
#pragma unroll
    for (int k = 0; k < 8; ++k) { pw_[k] = W + (r >> 3) * 8192 + (r & 7) * 256 + (((2 * k + h) ^ (r & 15)) * 16); PIN(pw_[k]); }
    spin_slow(lbase + L_PROG + 24, 1u);
    unsigned tag = __builtin_amdgcn_readfirstlane(peek(lbase + L_PROG + P_TAG));
    unsigned fo = (unsigned)min((22 * kq) / 3 + lane, (22 * kq + 21) / 3) * 128;
    const char* fbase = ws + WS_F2 + m2 * 4096;
    PIN(fo); asm volatile("" : "+s"(tag), "+s"(fbase));
    __builtin_amdgcn_s_setprio(2);
    spin_slow(lbase + L_PROG + 16, 9u);
    v4u we[7][2];
#pragma unroll
    for (int i = 0; i < 5; ++i) wfrag(we[i], i);
    spin(pa_c, 10u);
    wd_steps(5, 0);
    wd_steps(5, RS);
#pragma unroll
    for (int i = 5; i < 7; ++i) wfrag(we[i], i);
    wd_steps(5, 2 * RS);
#pragma unroll
    for (int i = 0; i < 7; ++i) { PIN(we[i][0]); PIN(we[i][1]); }
    waitl();
    wd_steps(2, 0);
    const __amdgpu_buffer_rsrc_t rh = __builtin_amdgcn_make_buffer_rsrc((void*)(ws + WS_H), 0, H_BYTES, 0x00020000);
    if (j == 15) spin_slow(pa_c, 20u);
    poll_eq(fbase, fo, tag);
    __builtin_amdgcn_s_setprio(0);
    v4u hf[HW][2];
    auto ldh = [&](int i) {
      int ii = i + rot2; ii = ii >= 22 ? ii - 22 : ii;
      const int F16 = fb + ii;
      const int so = ((m2 * 2 * 86 + F16) * 1024) | (((F16 - 22 * kq) >> 31) & 0x40000000);
      hf[i % HW][0] = __builtin_amdgcn_raw_buffer_load_b128(rh, lane * 16, so, 16);
      hf[i % HW][1] = __builtin_amdgcn_raw_buffer_load_b128(rh, lane * 16, so + 86 * 1024, 16);
    };
#pragma unroll
    for (int i = 0; i < HW0; ++i) ldh(i);
    sched0();
    f32x16 o2[2][2];
#pragma unroll
    for (int i = 0; i < 4; ++i) o2[i >> 1][i & 1] = f32x16{};
    v4u wn[2][2];
    constexpr int NT = 22 - TAILN;
    int nld = HW0;
#pragma unroll
    for (int i = 0; i < NT; ++i) {
      if (i + 1 >= 7 && i + 1 < NT) {
        wfrag(wn[(i + 1) & 1], i + 1 < 15 ? i + 1 : i + 1 - 15);
      }
      if (i >= NT - TAILN) {
        const int k = i - (NT - TAILN);
        wfrag(we[k], NT + k - 15);
      }
      const v4u w0 = i < 7 ? we[i < 7 ? i : 0][0] : wn[i & 1][0], w1 = i < 7 ? we[i < 7 ? i : 0][1] : wn[i & 1][1];
      const v4u h0 = hf[i % HW][0], h1 = hf[i % HW][1];
      o2[0][0] = mfma32(w0, h0, o2[0][0]); o2[1][0] = mfma32(w1, h0, o2[1][0]);
      o2[0][1] = mfma32(w0, h1, o2[0][1]); o2[1][1] = mfma32(w1, h1, o2[1][1]);
#pragma unroll
      for (int k = 0; k < 2; ++k) if (nld < 22 && nld < i + HW) { ldh(nld); ++nld; }
      if (i == YAT) prog_set(L, w, 14u);
      SGB(0x8, 1); SGB(0x100, 2); SGB(0x20, 1); SGB(0x8, 1); SGB(0x100, 2); SGB(0x20, 1); SGB(0x8, 1); SGB(0x100, 2); SGB(0x20, 1); SGB(0x8, 1); SGB(0x100, 2); SGB(0x20, 1);
      sched0();
    }
    asm volatile("" ::: "memory");
    auto wr = [&](int cc, int tt) {
#pragma unroll
      for (int g4 = 0; g4 < 4; ++g4)
        *(LAS f32x4*)(pw_[cc * 4 + g4] + tt * RS) = f32x4{o2[cc][tt][4 * g4], o2[cc][tt][4 * g4 + 1], o2[cc][tt][4 * g4 + 2], o2[cc][tt][4 * g4 + 3]};
    };
#pragma unroll
    for (int q = 0; q < 4; ++q) {
      const int cc = q & 1, tt = q >> 1;
#pragma unroll
      for (int k = 0; k < TAILN; ++k) o2[cc][tt] = mfma32(we[k][cc], hf[(NT + k) % HW][tt], o2[cc][tt]);
      if (q > 0) { wr((q - 1) & 1, (q - 1) >> 1); SGB(0x8, 1); SGB(0x200, 1); SGB(0x8, 1); SGB(0x200, 1); SGB(0x8, 1); SGB(0x200, 1); SGB(0x8, 1); SGB(0x200, 1); SGB(0x8, 2); }
      sched0();
    }
    wr(1, 1);
  }
  {
    const int tok = tid >> 3, c8 = tid & 7;
    LAS char* P = L + (tok >> 5) * RS + ((tok >> 3) & 3) * 8192 + (tok & 7) * 256;
    LAS char* p0 = P + (((2 * c8) ^ (tok & 15)) * 16);
    LAS char* p1 = P + (((2 * c8 + 1) ^ (tok & 15)) * 16);
    LAS char* pb = L + L_BASE + tok * 128 + ((c8 ^ (tok & 7)) * 16);
    unsigned oo = tok * 1024 + c8 * 16;
    char* ob = (char*)a.out + (size_t)(m2 * 64) * 1024 + n2 * 128;
    PIN(p0); PIN(p1); PIN(pb); PIN(oo); asm volatile("" : "+s"(ob));
    bar();
    asm volatile("" :: "s"(ka_touch));
    f32x4 q0[4], q1[4];
#pragma unroll
    for (int k = 0; k < 4; ++k) { q0[k] = *(LAS f32x4*)(p0 + k * 2048); q1[k] = *(LAS f32x4*)(p1 + k * 2048); }
    const v4u bv = *(LAS v4u*)pb;
    sched0();
    const f32x4 s0 = (q0[0] + q0[1]) + (q0[2] + q0[3]), s1 = (q1[0] + q1[1]) + (q1[2] + q1[3]);
    const unsigned f0_ = pk2(s0[0], s0[1]), f1_ = pk2(s0[2], s0[3]), f2_ = pk2(s1[0], s1[1]), f3_ = pk2(s1[2], s1[3]);
    const v4u o = {pk2(bflo(bv.x) + bflo(f0_), bfhi(bv.x) + bfhi(f0_)), pk2(bflo(bv.y) + bflo(f1_), bfhi(bv.y) + bfhi(f1_)),
                   pk2(bflo(bv.z) + bflo(f2_), bfhi(bv.z) + bfhi(f2_)), pk2(bflo(bv.w) + bflo(f3_), bfhi(bv.w) + bfhi(f3_))};
    asm volatile("global_store_dwordx4 %0, %1, %2 sc0 sc1\n\ts_nop 1" :: "v"(oo), "v"(o), "s"(ob) : "memory");
  }
  asm volatile(".Lfk_end:");
}

extern "C" void kernel_launch(void* const* d_in, const int* in_sizes, int n_in, void* d_out, int out_size, void* d_ws, size_t ws_size,
                              hipStream_t stream) {
  Args a;
  a.x = (const u16*)d_in[0]; a.are = (const u16*)d_in[1]; a.aim = (const u16*)d_in[2]; a.bre = (const u16*)d_in[3]; a.bim = (const u16*)d_in[4];
  a.cre = (const u16*)d_in[5]; a.cim = (const u16*)d_in[6]; a.logdt = (const u16*)d_in[7]; a.dsk = (const u16*)d_in[8];
  a.wg = (const u16*)d_in[10]; a.wu = (const u16*)d_in[11]; a.wd = (const u16*)d_in[12];
  a.out = (u16*)d_out; a.ws = (char*)d_ws;
  (void)hipFuncSetAttribute((const void*)fused_kernel, hipFuncAttributeMaxDynamicSharedMemorySize, L_TOTAL);
  fused_kernel<<<256, 512, L_TOTAL, stream>>>(a);
}
```

```cpp
#include <hip/hip_runtime.h>
#include <hip/hip_bf16.h>

typedef unsigned short u16;
typedef unsigned v4u __attribute__((ext_vector_type(4)));
typedef unsigned v2u __attribute__((ext_vector_type(2)));
typedef short s16x4 __attribute__((ext_vector_type(4)));
typedef __bf16 bf16x8 __attribute__((ext_vector_type(8)));
typedef __bf16 bf16x2 __attribute__((ext_vector_type(2)));
typedef float f32x2 __attribute__((ext_vector_type(2)));
typedef float f32x4 __attribute__((ext_vector_type(4)));
typedef float f32x16 __attribute__((ext_vector_type(16)));
#define LAS __attribute__((address_space(3)))
#define GAS __attribute__((address_space(1)))
#define DEVI __device__ __forceinline__

#define STAMPS 0
#define STW 7
#define LPRIO 1
#define XTS 7u
#define MAPB 1
#define IDLEW 6
#define YDEL 8
#define NOSEL 0
#define SLP "2"
#define CPRIO 0
#define PACE 3
#define PACEW 0
#define ORD 1
#define LNO 0
#define CONVAT 12u
#define CONVOWN 1
#define VPRIO 2
#define EPRIO 1
#define YAT 12
#define YSL 6
#define WDPF 0
#define PSL 1
#define MAPQ 0
#define W3AT 10u
#define WDB 0
#define W1AT 7u
#define PUBAT -1
#define GPRIO 2
#define OUTM 3
#define HW0 4
#define TAILN 6
#define BDEL 3
#define ABLW 0
#define CAP 12
#define XLATE 1
#define ADEL 0
#define HOLE 1
#define XROT 3
#define SKIP14 1
#define LSL 0
#define PW8 0
#define P9AT 10u
#define CAPW 63
#define WWIN 4
#define TW0 1
#define FASTS 0
#define P2 1
#define SERP 0
#define ROT1 1

constexpr int DM = 512, FF = 1376;
constexpr int WS_EPOCH = 0;
constexpr int WS_F1 = 4096;
constexpr int WS_F2 = 1 << 18;
constexpr int WS_STAT = 16384;
constexpr int WS_STAMP = 65536;
constexpr int WS_XT = 1 << 20;
constexpr int WS_YT = 3 << 20;
constexpr int WS_H = 7 << 20;
constexpr int H_BYTES = 64 * 86 * 1024;
constexpr int RS = 41024, WGO = 16384, WUO = 16384 + 12320;
constexpr int CS = 4160;
constexpr int L_KIMG = 3 * RS;
constexpr int L_UIMG = L_KIMG + 4 * CS;
constexpr int US = 4608;
constexpr int L_ZT = L_KIMG, ZTS = 10064;
constexpr int L_CSUM = L_KIMG + 36864;
constexpr int L_PROG = L_ZT + 4 * ZTS;
constexpr int L_TOTAL = L_PROG + 64;
static_assert(L_TOTAL <= 163840, "lds");
DEVI int part_off(int w) { return L_ZT + (w == 2 ? 25856 : w * ZTS); }
constexpr int LNS = 1088;
constexpr int L_STAT = L_KIMG + 34304;
constexpr int L_BASE = L_KIMG + 25856;
constexpr unsigned SPIN_CAP = 1u << 17;
constexpr int wd_pos(int i) { return ((i / 5 + 2) % 3) * RS + (i % 5) * 8192; }

DEVI float bf2f(unsigned b) { return __builtin_bit_cast(float, b << 16); }
DEVI float bflo(unsigned w) { return __builtin_bit_cast(float, w << 16); }
DEVI float bfhi(unsigned w) { return __builtin_bit_cast(float, w & 0xffff0000u); }
DEVI unsigned pk2v(f32x2 v) { return __builtin_bit_cast(unsigned, __builtin_convertvector(v, bf16x2)); }
DEVI unsigned pk2(float a, float b) { return pk2v(f32x2{a, b}); }
DEVI float rbf(float a) { return bflo(pk2(a, a)); }
DEVI f32x16 mfma32(v4u a, v4u b, f32x16 c) { return __builtin_amdgcn_mfma_f32_32x32x16_bf16(__builtin_bit_cast(bf16x8, a), __builtin_bit_cast(bf16x8, b), c, 0, 0, 0); }
DEVI f32x4 mfma16(v4u a, v4u b, f32x4 c) { return __builtin_amdgcn_mfma_f32_16x16x32_bf16(__builtin_bit_cast(bf16x8, a), __builtin_bit_cast(bf16x8, b), c, 0, 0, 0); }
DEVI v2u trd(LAS char* p) { return __builtin_bit_cast(v2u, __builtin_amdgcn_ds_read_tr16_b64_v4i16((LAS s16x4*)p)); }
DEVI v4u trd2(LAS char* p0, LAS char* p1) { v2u a = trd(p0), b = trd(p1); return v4u{a.x, a.y, b.x, b.y}; }
template <int N> DEVI void waitv() { asm volatile("s_waitcnt vmcnt(%0)" :: "i"(N) : "memory"); }
DEVI void waitl() { asm volatile("" ::: "memory"); __builtin_amdgcn_s_waitcnt(0xC07F); asm volatile("" ::: "memory"); }
DEVI void bar() { waitl(); asm volatile("s_waitcnt lgkmcnt(0)\n\ts_barrier" ::: "memory"); }
DEVI void sched0() { __builtin_amdgcn_sched_barrier(0); }
#define SGB(mask, n) __builtin_amdgcn_sched_group_barrier(mask, n, 0)
DEVI void ld16(v4u& r, const void* sbase, unsigned voff) { asm volatile("global_load_dwordx4 %0, %1, %2" : "=v"(r) : "v"(voff), "s"(sbase) : "memory"); }
DEVI void ld16c(v4u& r, const void* sbase, unsigned voff) { asm volatile("global_load_dwordx4 %0, %1, %2 sc1" : "=v"(r) : "v"(voff), "s"(sbase) : "memory"); }
DEVI void ld8c(v2u& r, const void* sbase, unsigned voff) { asm volatile("global_load_dwordx2 %0, %1, %2 sc1" : "=v"(r) : "v"(voff), "s"(sbase) : "memory"); }
DEVI void ld4c(unsigned& r, const void* sbase, unsigned voff) { asm volatile("global_load_dword %0, %1, %2 sc1" : "=v"(r) : "v"(voff), "s"(sbase) : "memory"); }
DEVI void ld2(unsigned& r, const void* sbase, unsigned voff) { asm volatile("global_load_ushort %0, %1, %2" : "=v"(r) : "v"(voff), "s"(sbase) : "memory"); }
DEVI void st16c(void* sbase, unsigned voff, v4u v) { asm volatile("global_store_dwordx4 %0, %1, %2 sc1\n\ts_nop 1" :: "v"(voff), "v"(v), "s"(sbase) : "memory"); }
DEVI void st8c(void* sbase, unsigned voff, v2u v) { asm volatile("global_store_dwordx2 %0, %1, %2 sc1\n\ts_nop 1" :: "v"(voff), "v"(v), "s"(sbase) : "memory"); }
DEVI void st4c(void* sbase, unsigned voff, unsigned v) { asm volatile("global_store_dword %0, %1, %2 sc1\n\ts_nop 1" :: "v"(voff), "v"(v), "s"(sbase) : "memory"); }
DEVI void flag1(void* sbase, unsigned voff, unsigned v) {
  asm volatile("s_mov_b64 exec, 1\n\tglobal_store_dword %0, %1, %2 sc1\n\ts_mov_b64 exec, -1" :: "v"(voff), "v"(v), "s"(sbase) : "memory");
}
#define PIN(x) asm volatile("" : "+v"(x))
template <int C> DEVI float dppf(float v) { return __builtin_bit_cast(float, __builtin_amdgcn_update_dpp(0, __builtin_bit_cast(int, v), C, 0xf, 0xf, true)); }
DEVI float rsum16(float v) { v += dppf<0xB1>(v); v += dppf<0x4E>(v); v += dppf<0x141>(v); v += dppf<0x140>(v); return v; }
DEVI float cut(float x) { asm("" : "+v"(x)); return x; }
DEVI unsigned pk2s(float a, float b) { return pk2(cut(a), cut(b)); }
DEVI float dot2(unsigned a, unsigned b, float c) { return __builtin_amdgcn_fdot2_f32_bf16(__builtin_bit_cast(bf16x2, a), __builtin_bit_cast(bf16x2, b), c, false); }
DEVI void dma2(const void* sbase, unsigned v0, unsigned v1, unsigned ldsaddr) {
  asm volatile("s_mov_b32 m0, %3\n\ts_nop 0\n\tglobal_load_lds_dwordx4 %0, %2\n\tglobal_load_lds_dwordx4 %1, %2 offset:1024" :: "v"(v0), "v"(v1), "s"(sbase), "s"(ldsaddr) : "memory");
}
#define DMA4(a, b, c, d, sb, lb, off, p, q) \
  "s_waitcnt vmcnt(%" #q ")\n\ts_add_i32 m0, %" #lb ", " #off "\n\ts_nop %" #p "\n\tglobal_load_lds_dwordx4 %" #a ", %" #sb "\n\ts_nop %" #p "\n\tglobal_load_lds_dwordx4 %" #b ", %" #sb " offset:1024\n\t" \
  "s_nop %" #p "\n\tglobal_load_lds_dwordx4 %" #c ", %" #sb " offset:2048\n\ts_nop %" #p "\n\tglobal_load_lds_dwordx4 %" #d ", %" #sb " offset:3072\n\t"
template <int P> DEVI void dma8(const void* sbase, const unsigned* v, unsigned lb) {
  asm volatile(DMA4(0, 1, 2, 3, 8, 9, 0, 10, 11) DMA4(4, 5, 6, 7, 8, 9, 4096, 10, 11)
               :: "v"(v[0]), "v"(v[1]), "v"(v[2]), "v"(v[3]), "v"(v[4]), "v"(v[5]), "v"(v[6]), "v"(v[7]), "s"(sbase), "s"(lb), "i"(P), "i"(P == PACE ? CAP : P ? CAPW : 63) : "memory", "scc");
}
template <int P> DEVI void dma12(const void* sbase, const unsigned* v, unsigned lb) {
  asm volatile(DMA4(0, 1, 2, 3, 12, 13, 0, 14, 15) DMA4(4, 5, 6, 7, 12, 13, 4096, 14, 15) DMA4(8, 9, 10, 11, 12, 13, 8192, 14, 15)
               :: "v"(v[0]), "v"(v[1]), "v"(v[2]), "v"(v[3]), "v"(v[4]), "v"(v[5]), "v"(v[6]), "v"(v[7]), "v"(v[8]), "v"(v[9]), "v"(v[10]), "v"(v[11]),
                  "s"(sbase), "s"(lb), "i"(P), "i"(P == PACE ? CAP : P ? CAPW : 63) : "memory", "scc");
}
template <int ST> DEVI void stouch16(const void* p) {
  unsigned t;
  asm volatile(".irp k,0,1,2,3,4,5,6,7,8,9,10,11,12,13,14,15\n\ts_load_dword %0, %1, \\k*%c2\n\t.endr\n\ts_waitcnt lgkmcnt(0)" : "=&s"(t) : "s"(p), "i"(ST) : "memory");
}
DEVI void touch(const void* sbase, unsigned voff, unsigned ldsdump) {
  asm volatile("s_mov_b32 m0, %2\n\ts_nop 0\n\tglobal_load_lds_dword %0, %1" :: "v"(voff), "s"(sbase), "s"(ldsdump) : "memory");
}
#define CMM(r, a, x) "v_pk_mul_f32 %" #r ", %" #a ", %" #x " op_sel:[1,1] op_sel_hi:[1,0]\n\t"
#define CMF(r, a, x) "v_pk_fma_f32 %" #r ", %" #a ", %" #x ", %" #r " op_sel_hi:[0,1,1] neg_lo:[0,0,1]\n\t"
DEVI void cm4(f32x2& r0, f32x2& r1, f32x2& r2, f32x2& r3, f32x2 a0, f32x2 x0, f32x2 a1, f32x2 x1, f32x2 a2, f32x2 x2, f32x2 a3, f32x2 x3) {
  asm(CMM(0, 4, 5) CMM(1, 6, 7) CMM(2, 8, 9) CMM(3, 10, 11) CMF(0, 4, 5) CMF(1, 6, 7) CMF(2, 8, 9) CMF(3, 10, 11)
      : "=&v"(r0), "=&v"(r1), "=&v"(r2), "=&v"(r3) : "v"(a0), "v"(x0), "v"(a1), "v"(x1), "v"(a2), "v"(x2), "v"(a3), "v"(x3));
}
DEVI void put1(unsigned addr, unsigned v) {
  asm volatile("s_mov_b64 exec, 1\n\tds_write_b32 %0, %1\n\ts_mov_b64 exec, -1" :: "v"(addr), "v"(v) : "memory");
}
DEVI void prog_set(LAS char* L, int w, unsigned v) { put1((unsigned)(size_t)L + L_PROG + w * 4, v); }
DEVI void prog_done(LAS char* L, int w, unsigned v) { waitl(); prog_set(L, w, v); }
DEVI unsigned paddr(unsigned lbase, int lane, int w0, int mask) { return lbase + L_PROG + (w0 + (lane & mask)) * 4; }
DEVI void spin(unsigned addr, unsigned n) {
  unsigned t, c = 1u << 22;
  asm volatile("1:\n\tds_read_b32 %0, %2\n\ts_sub_u32 %1, %1, 1\n\ts_waitcnt lgkmcnt(0)\n\tv_cmp_gt_u32 vcc, %3, %0\n\ts_cselect_b64 vcc, 0, vcc\n\ts_cbranch_vccnz 1b"
               : "=&v"(t), "+s"(c) : "v"(addr), "s"(n) : "memory", "vcc", "scc");
}
DEVI void spin_if(unsigned seen, unsigned addr, unsigned n) {
  unsigned t, c;
  asm volatile("s_cmp_ge_u32 %4, %3\n\ts_cbranch_scc1 2f\n\ts_mov_b32 %1, 0x400000\n"
               "1:\n\tds_read_b32 %0, %2\n\ts_sub_u32 %1, %1, 1\n\ts_waitcnt lgkmcnt(0)\n\tv_cmp_gt_u32 vcc, %3, %0\n\ts_cselect_b64 vcc, 0, vcc\n\ts_cbranch_vccnz 1b\n2:"
               : "=&v"(t), "=&s"(c) : "v"(addr), "s"(n), "s"(seen) : "memory", "vcc", "scc");
}
DEVI void spin_slow(unsigned addr, unsigned n) {
  unsigned t, c = 1u << 21;
  asm volatile("1:\n\tds_read_b32 %0, %2\n\ts_waitcnt lgkmcnt(0)\n\tv_cmp_gt_u32 vcc, %3, %0\n\ts_cbranch_vccz 2f\n\ts_sleep " SLP "\n\ts_sub_u32 %1, %1, 1\n\ts_cbranch_scc0 1b\n2:"
               : "=&v"(t), "+s"(c) : "v"(addr), "s"(n) : "memory", "vcc", "scc");
}
DEVI void poll_eq(const void* sbase, unsigned voff, unsigned want) {
  unsigned t, c = SPIN_CAP;
  asm volatile("1:\n\tglobal_load_dword %0, %2, %3 sc1\n\ts_sub_u32 %1, %1, 1\n\ts_waitcnt vmcnt(0)\n\tv_cmp_ne_u32 vcc, %4, %0\n\ts_cselect_b64 vcc, 0, vcc\n\ts_cbranch_vccnz 1b"
               : "=&v"(t), "+s"(c) : "v"(voff), "s"(sbase), "s"(want) : "memory", "vcc", "scc");
}
DEVI unsigned peek(unsigned addr) { return *(volatile LAS unsigned*)(size_t)addr; }
#define STAMP(i)
#define STAMP5(i)
#define STAMP3(i)

#define STAMP4(s, k)
template <int N> struct IC { static constexpr int value = N; };
struct Args {
  const u16 *x, *are, *aim, *bre, *bim, *cre, *cim, *logdt, *dsk, *wg, *wu, *wd;
  u16* out;
  char* ws;
};

__global__ void __launch_bounds__(512, 2) fused_kernel(Args a) {
  extern __shared__ __attribute__((aligned(16))) char smem[];
  LAS char* L = (LAS char*)smem;
  const unsigned lbase = (unsigned)(size_t)L;
  STAMP(0);
  unsigned ka_touch;
  asm volatile("s_load_dword %0, %1, 0x0" : "=s"(ka_touch) : "s"(__builtin_amdgcn_kernarg_segment_ptr()));
  unsigned long long pc; unsigned sz;
  asm volatile("s_getpc_b64 %0\n.Lfk_start:\n\ts_mov_b32 %1, .Lfk_end-.Lfk_start\n\ts_max_u32 %1, %1, .Lfk_e1-.Lfk_start\n\ts_max_u32 %1, %1, .Lfk_e2-.Lfk_start\n\ts_max_u32 %1, %1, .Lfk_e3-.Lfk_start"
               : "=s"(pc), "=s"(sz) :: "scc");
  const int tid = threadIdx.x, lane = tid & 63;
  const int w = __builtin_amdgcn_readfirstlane(tid >> 6);
  const int hw = w & 3;
  const int b = blockIdx.x, xcd = b & 7, slot = b >> 3;
  unsigned codetouch = 0;
  {
    const unsigned off = (unsigned)((lane * 8 + (slot & 7)) * 128);
    if (w == 3 && off < sz) asm volatile("global_load_dword %0, %1, %2" : "=v"(codetouch) : "v"(off), "s"(pc) : "memory");
  }
  prog_set(L, w, 0u); prog_set(L, w + 8, 0u);
  const int tl = 30 * (xcd & 3) + slot;
  const int m = (xcd >> 2) * 8 + (tl & 7), j = slot < 30 ? tl >> 3 : 15;
  const int jl = (xcd & 3) * 4 + (slot & 3);
  const int f0 = min(96 * j, FF - 96);
  const int m2 = xcd * 4 + (slot >> 3), n2 = slot & 7;
  const int t0 = ((xcd >> 2) * 8 + (slot >> 2)) * 128 + (jl >> 1) * 16;
  const int i16 = lane & 15, g = lane >> 4;
  char* ws = a.ws;
  const int rot2 = ((n2 * 11) / 4 + ((slot >> 3) * 11) / 2 + xcd * XROT) % 22;
  const unsigned vd0 = (lane >> 3) * 1024 + (((lane & 7) ^ ((g & 1) << 2)) * 16);
  const unsigned pa_c = paddr(lbase, lane, 0, 3);

  if (w < 4) {
    if (CPRIO) __builtin_amdgcn_s_setprio(CPRIO);
    unsigned pq[7], epoch_v;
    {
      const unsigned po = (unsigned)(b * 256 + tid) * 2;
      ld2(pq[6], a.logdt, (unsigned)(2 * b + (w >> 1)) * 2);
      ld2(pq[0], a.are, po); ld2(pq[1], a.aim, po); ld2(pq[2], a.bre, po); ld2(pq[3], a.bim, po); ld2(pq[4], a.cre, po); ld2(pq[5], a.cim, po);
      ld4c(epoch_v, ws + WS_EPOCH, 0u);
    }
    bar();
    const int tg = w & 1, fh = w >> 1;
    const int r = lane & 31, h = lane >> 5;
    const unsigned one2 = ((i16 & 7) == 4 * (g & 1)) ? 0x3F803F80u : 0u;
    v4u sel = {one2, one2, one2, one2};
    int xo[4], wo[2];
#pragma unroll
    for (int p = 0; p < 4; ++p) xo[p] = (tg * 64 + r) * 128 + (((2 * (p ^ (2 * tg)) + h) ^ ((r >> 1) & 7)) * 16);
    wo[0] = WGO + (g & 1) * 12320 + (8 * h + (i16 >> 2)) * 192 + fh * 96 + (lane & 3) * 8;
    wo[1] = wo[0] + (1 - tg) * 6144;
    wo[0] += tg * 6144;
    int zrow;
    {
      const int e = (r & 3) + 4 * (r >> 3), hh = (r >> 2) & 1;
      const unsigned long long lo = hh ? 0x10191818181709ull << 8 | 0x09ull : 0x0706050403020100ull;
      const unsigned long long hi = hh ? 0x160F150E121B0B10ull : 0x140D130C111A0A08ull;
      zrow = (int)(((e < 8 ? lo : hi) >> (8 * (e & 7))) & 31);
    }
    int zc_ = L_ZT + w * ZTS + zrow * 272 + h * 16, zf_ = L_ZT + w * ZTS + r * 272 + h * 16;
    const unsigned pa_l = lbase + L_PROG + 16;
#pragma unroll
    for (int p = 0; p < 4; ++p) PIN(xo[p]);
    PIN(wo[0]); PIN(wo[1]); PIN(zc_); PIN(zf_); PIN(sel);
    waitv<0>();
#pragma unroll
    for (int e = 0; e < 7; ++e) PIN(pq[e]);
    PIN(codetouch); PIN(epoch_v);
    const unsigned tag = __builtin_amdgcn_readfirstlane(epoch_v) * 0x9E3779B1u + 0x7F4A7C15u;
    STAMP(1);
    {
      const float dt = rbf(expf(bf2f(pq[6])));
      const float ar = (dt * bf2f(pq[0])) * 1.44269504088896341f;
      float ai = (dt * bf2f(pq[1])) * 0.15915494309189535f;
      ai -= __builtin_rintf(ai);
      const float br = dt * bf2f(pq[2]), bi = dt * bf2f(pq[3]), cr0 = bf2f(pq[4]), ci0 = bf2f(pq[5]);
      f32x2 C = {cr0 * br - ci0 * bi, -(cr0 * bi + ci0 * br)};
      const f32x2 zc = f32x2{__builtin_amdgcn_cosf(ai), -__builtin_amdgcn_sinf(ai)} * __builtin_amdgcn_exp2f(ar);
      const f32x2 u8 = f32x2{__builtin_amdgcn_cosf(ai * 8.f), __builtin_amdgcn_sinf(ai * 8.f)} * __builtin_amdgcn_exp2f(ar * 8.f);
      f32x2 b0 = f32x2{__builtin_amdgcn_cosf(ai * 128.f), __builtin_amdgcn_sinf(ai * 128.f)} * __builtin_amdgcn_exp2f(ar * 128.f);
      const f32x2 u8c = {u8.x, -u8.y}, b0c = {b0.x, -b0.y};
      LAS unsigned* zt = (LAS unsigned*)(L + L_ZT + w * ZTS + lane * 4);
      f32x2 vc, E, b1, u16_, D = C, ex = C;
      cm4(vc, E, b1, u16_, b0c, b0c, C, u8c, b0, u8, u8, u8);
      zt[0] = pk2v(C); zt[8 * 68] = pk2v(E);
      f32x2 c6 = C;
#pragma unroll
      for (int k = 1; k < 8; ++k) {
        if (k == 3 || k == 5) ex = C;
        if (k == 7) c6 = C;
        cm4(C, D, E, ex, C, zc, D, vc, E, vc, ex, vc);
        zt[k * 68] = pk2v(C); zt[(8 + k) * 68] = pk2v(D); zt[(15 + k) * 68] = pk2v(E);
        if (k == 3) zt[23 * 68] = pk2v(ex);
        if (k == 5) zt[24 * 68] = pk2v(ex);
        if (k == 6) zt[26 * 68] = pk2v(ex);
        if (k == 7) zt[27 * 68] = pk2v(ex);
      }
      f32x2 a0 = u16_, a1, b2, b3;
      cm4(c6, a1, b2, b3, c6, vc, u8, u16_, b0, u16_, b1, u16_);
      zt[25 * 68] = pk2v(c6);
      asm volatile("" ::: "memory");
      v4u fc[8], ff[8];
#pragma unroll
      for (int ks = 0; ks < 8; ++ks) fc[ks] = *(LAS v4u*)(L + zc_ + ks * 32);
      asm volatile("" ::: "memory");
      zt[0] = 0x00003F80u; zt[68] = pk2v(u8); zt[16 * 68] = pk2v(b0); zt[17 * 68] = pk2v(b1);
      zt[2 * 68] = pk2v(a0); zt[3 * 68] = pk2v(a1); zt[18 * 68] = pk2v(b2); zt[19 * 68] = pk2v(b3);
#pragma unroll
      for (int k = 2; k < 8; ++k) {
        cm4(a0, a1, b2, b3, a0, u16_, a1, u16_, b2, u16_, b3, u16_);
        zt[2 * k * 68] = pk2v(a0); zt[(2 * k + 1) * 68] = pk2v(a1); zt[(16 + 2 * k) * 68] = pk2v(b2); zt[(17 + 2 * k) * 68] = pk2v(b3);
      }
      asm volatile("" ::: "memory");
#pragma unroll
      for (int ks = 0; ks < 8; ++ks) ff[ks] = *(LAS v4u*)(L + zf_ + ks * 32);
      sched0();
      f32x16 kd = {};
#pragma unroll
      for (int ks = 0; ks < 8; ++ks) kd = mfma32(fc[ks], ff[ks], kd);
#pragma unroll
      for (int g4 = 0; g4 < 4; ++g4) *(LAS f32x4*)(L + part_off(w) + g4 * 1024 + lane * 16) = f32x4{kd[4 * g4], kd[4 * g4 + 1], kd[4 * g4 + 2], kd[4 * g4 + 3]};
      asm volatile("" ::: "memory");
      prog_set(L, w, 1u);
    }
    STAMP(2);
    if (2 * j - fh < (SKIP14 ? 28 : 29)) {
      f32x16 acc[2][3];
#pragma unroll
      for (int i = 0; i < 2; ++i)
#pragma unroll
        for (int c = 0; c < 3; ++c) acc[i][c] = f32x16{};
      f32x4 cs[3] = {};
      v4u xf[2][2], wf[2][3];
      auto ldf = [&](int buf, int sl, int p) {
        LAS char* S = L + sl * RS;
        LAS char* Wp = S + wo[p >> 1] + (p & 1) * 3072;
        wf[buf][0] = trd2(Wp, Wp + 768);
        xf[buf][0] = *(LAS v4u*)(S + xo[p]);
        xf[buf][1] = *(LAS v4u*)(S + xo[p] + 4096);
        wf[buf][1] = trd2(Wp + 32, Wp + 800);
        wf[buf][2] = trd2(Wp + 64, Wp + 832);
      };
      auto mmc = [&](int buf, int c) { const int f = SERP ? (c & 1) : 0; acc[f][c] = mfma32(wf[buf][c], xf[buf][f], acc[f][c]); acc[f ^ 1][c] = mfma32(wf[buf][c], xf[buf][f ^ 1], acc[f ^ 1][c]); };
      auto mm = [&](int buf, bool dosel) {
        mmc(buf, 0); mmc(buf, 1); mmc(buf, 2);
        if (dosel && !NOSEL) {
#pragma unroll
          for (int c = 0; c < 3; ++c) cs[c] = mfma16(sel, wf[buf][c], cs[c]);
        }
      };
      auto pattern = [&]() { SGB(0x8, 1); SGB(0x100, 2); SGB(0x8, 1); SGB(0x100, 2); SGB(0x8, 1); SGB(0x100, 2); SGB(0x8, 1); SGB(0x100, 3); SGB(0x8, 5); sched0(); };
      spin(pa_l, 1u);
      STAMP(3); STAMP3(0);
      ldf(0, 0, 0);
      sched0();
      unsigned pw = lbase + L_PROG + w * 4;
      unsigned pl[2] = {pa_l, pa_l + 4};
      PIN(pw); PIN(pl[0]); PIN(pl[1]);
#pragma unroll
      for (int s = 0; s < 7; ++s) {
        const int sl = s % 3;
        ldf(1, sl, 1); mm(0, true); pattern();
        STAMP4(s, 0);
        ldf(0, sl, 2);
        const unsigned full = peek(pl[(s + 1) & 1]);
        unsigned pv = s + 2;
        PIN(pv);
        mm(1, true); pattern();
        STAMP4(s, 1);
        ldf(1, sl, 3);
        unsigned fs = __builtin_amdgcn_readfirstlane(full);
        mmc(0, 0); mmc(0, 1);
        SGB(0x8, 1); SGB(0x100, 2); SGB(0x2, 1); SGB(0x8, 1); SGB(0x100, 2); SGB(0x8, 1); SGB(0x100, 2); SGB(0x8, 1); SGB(0x100, 2);
        sched0();
        if (s == 6) {
#pragma unroll
          for (int c = 0; c < 3; ++c) *(LAS float*)(L + L_CSUM + w * 768 + c * 256 + lane * 4) = cs[c][0];
          asm volatile("" ::: "memory");
        }
        put1(pw, pv);
        sched0();
        mmc(0, 2);
        sched0();
        STAMP4(s, 2);
        STAMP3(2 * s + 1);
        spin_if(fs, pl[(s + 1) & 1], s + 2);
        asm volatile("" ::: "memory");
        STAMP3(2 * s + 2);
        STAMP4(s, 3);
        sched0();
        ldf(0, (s + 1) % 3, 0); mm(1, false); pattern();
        STAMP4(s, 4);
      }
      const unsigned pq_ = peek(lbase + L_PROG + 24);
      ldf(1, 1, 1);
      const unsigned pp_ = peek(lbase + L_PROG + (w ^ 1) * 4);
      mmc(0, 0); mmc(0, 1);
      SGB(0x8, 1); SGB(0x100, 3); SGB(0x8, 1); SGB(0x100, 3); SGB(0x8, 1); SGB(0x100, 2); SGB(0x8, 1); SGB(0x100, 2); sched0();
      const unsigned ws_ = __builtin_amdgcn_readfirstlane(pq_);
      mmc(0, 2);
#pragma unroll
      for (int c = 0; c < 3; ++c) cs[c] = mfma16(sel, wf[0][c], cs[c]);
      sched0();
      spin_if(ws_, lbase + L_PROG + 24, 3u);
      sched0();
      const unsigned sv[2] = {*(LAS unsigned*)(L + L_STAT + (tg * 64 + r) * 4), *(LAS unsigned*)(L + L_STAT + (tg * 64 + 32 + r) * 4)};
      ldf(0, 1, 2);
      const unsigned ps_ = __builtin_amdgcn_readfirstlane(pp_);
      float c1[2], ir2[2];
      v4u xa[2], wa[3];
#pragma unroll
      for (int i = 0; i < 2; ++i) {
        const unsigned nm = h ? 0u : (sv[i] & 0xffffu) ^ 0x8000u;
        xa[i] = v4u{nm | (nm << 16), nm, 0u, 0u};
        const float sd = bfhi(sv[i]);
        c1[i] = cut(-1.44269504088896341f * __builtin_amdgcn_rcpf(sd)); ir2[i] = cut(sd * sd);
        asm("" : "+v"(xa[i]));
      }
      mm(1, true);
      SGB(0x8, 1); SGB(0x100, 3); SGB(0x2, 2); SGB(0x8, 1); SGB(0x100, 3); SGB(0x8, 1); SGB(0x100, 2); SGB(0x8, 1); SGB(0x100, 2); SGB(0x2, 3);
      SGB(0x8, 1); SGB(0x2, 5); SGB(0x8, 1); SGB(0x2, 5); SGB(0x8, 1); SGB(0x2, 2); SGB(0x8, 1); SGB(0x2, 2); SGB(0x8, 1); sched0();
      spin_if(ps_, lbase + L_PROG + (w ^ 1) * 4, 8u);
      asm volatile("" ::: "memory");
      sched0();
      ldf(1, 1, 3);
      float csp[3];
#pragma unroll
      for (int c = 0; c < 3; ++c) csp[c] = *(LAS float*)(L + L_CSUM + (w ^ 1) * 768 + c * 256 + lane * 4);
      mmc(0, 0); mmc(0, 1);
      SGB(0x8, 1); SGB(0x100, 3); SGB(0x8, 1); SGB(0x100, 3); SGB(0x8, 1); SGB(0x100, 3); SGB(0x8, 1); SGB(0x100, 2); sched0();
      if (EPRIO) __builtin_amdgcn_s_setprio(EPRIO);
      put1(pw, 10u);
      sched0();
      mmc(0, 2);
#pragma unroll
      for (int c = 0; c < 3; ++c) cs[c] = mfma16(sel, wf[0][c], cs[c]);
#pragma unroll
      for (int c = 0; c < 3; ++c) cs[c] = mfma16(sel, wf[1][c], cs[c]);
      sched0();
      STAMP(4); STAMP3(15);
      mmc(1, 0);
      sched0();
#pragma unroll
      for (int c = 0; c < 3; ++c) {
        const float s = cut(cs[c][0] + csp[c]);
        const float hi = bfhi(__builtin_bit_cast(unsigned, s)), d1 = cut(s - hi), mid = bfhi(__builtin_bit_cast(unsigned, d1)), lo = cut(d1 - mid);
        wa[c] = v4u{__builtin_amdgcn_perm(__builtin_bit_cast(unsigned, d1), __builtin_bit_cast(unsigned, s), 0x07060302u), __builtin_bit_cast(unsigned, lo) >> 16, 0u, 0u};
      }
      mmc(1, 1); mmc(1, 2);
      SGB(0x2, 5); SGB(0x8, 1); SGB(0x2, 6); SGB(0x8, 1); SGB(0x2, 6); SGB(0x8, 1); SGB(0x2, 6); SGB(0x8, 1); sched0();
#pragma unroll
      for (int c = 0; c < 3; ++c) { acc[0][c] = mfma32(wa[c], xa[0], acc[0][c]); acc[1][c] = mfma32(wa[c], xa[1], acc[1][c]); }
      STAMP(5);
      const __amdgpu_buffer_rsrc_t rhs = __builtin_amdgcn_make_buffer_rsrc((void*)(ws + WS_H), 0, H_BYTES, 0x00020000);
#pragma unroll
      for (int c = 0; c < 3; ++c) {
        const int F16 = f0 / 16 + fh * 3 + c;
#pragma unroll
        for (int i = 0; i < 2; ++i) {
          float hv[8];
#pragma unroll
          for (int e = 0; e < 8; ++e) {
            const float gv = acc[i][c][e], u = acc[i][c][e + 8];
            const float ex = __builtin_amdgcn_exp2f(gv * c1[i]);
            hv[e] = (gv * u) * __builtin_amdgcn_rcpf(ex * ir2[i] + ir2[i]);
          }
          const v4u o = {pk2(hv[0], hv[1]), pk2(hv[2], hv[3]), pk2(hv[4], hv[5]), pk2(hv[6], hv[7])};
          const int so = (((m * 4 + tg * 2 + i) * 86 + F16) * 1024) | (((F16 - 6 * j) >> 31) & 0x40000000);
          __builtin_amdgcn_raw_buffer_store_b128(o, rhs, lane * 16, so, 16);
          asm volatile("s_nop 1");
          sched0();
        }
      }
    }
    prog_set(L, w, 12u);
    STAMP(6); STAMP5(0);
    const int cl = w >> 1, rh_ = w & 1;
    f32x4 y[8];
#pragma unroll
    for (int t = 0; t < 8; ++t) y[t] = f32x4{};
    {
      const int s0 = 2031 - i16 + 8 * g - rh_ * 128 - 96;
      LAS char* ka = L + L_KIMG + cl * 2 * CS + (s0 & 1) * CS + ((s0 + 1) >> 1) * 4;
      LAS char* ub = L + L_UIMG + cl * US + 512 + (16 * i16 - 16 + 8 * g - rh_ * 128 - 96) * 2;
      LAS char* kq_[4];
#pragma unroll
      for (int q = 0; q < 4; ++q) { kq_[q] = ka - (2 * q + 1) * 512; asm volatile("" : "+v"(kq_[q])); }
      v4u fa[2][8], fb[2][8];
      auto ldc = [&](int buf, int rr_) {
#pragma unroll
        for (int q = 0; q < 8; ++q) {
          LAS unsigned* p = (LAS unsigned*)(kq_[q >> 1] + ((q & 1) ? 0 : 512) + (3 - rr_) * 64);
          fa[buf][q] = v4u{p[0], p[1], p[2], p[3]};
          fb[buf][q] = *(LAS v4u*)(ub + q * 512 + (3 - rr_) * 64);
        }
      };
      auto publish = [&]() {
        waitv<0>();
        if (j < 15) flag1(ws + WS_F2, (unsigned)(((m * 2 + tg) * 16 + j) * 2 + fh) * 128, tag);
        prog_set(L, w, 13u);
      };
      const unsigned pa_w = paddr(lbase, lane, 6, 1);
      bool early = true;
      if (__any(peek(pa_w) < 5u)) { publish(); early = false; spin_slow(pa_w, 5u); }
      ldc(0, 0);
      sched0();
      if (PUBAT < 0) { if (early) publish(); STAMP(7); sched0(); }
#pragma unroll
      for (int rr_ = 0; rr_ < 4; ++rr_) {
        if (rr_ < 3) ldc((rr_ + 1) & 1, rr_ + 1);
#pragma unroll
        for (int d = 0; d < 8; ++d)
#pragma unroll
          for (int o = 0; o <= d; ++o) {
            if (d + o < 8) y[d + o] = mfma16(fa[rr_ & 1][d], fb[rr_ & 1][o], y[d + o]);
            if (o < d && d + o < 8) y[d + o] = mfma16(fa[rr_ & 1][o], fb[rr_ & 1][d], y[d + o]);
          }
        if (rr_ < 3) {
#pragma unroll
          for (int k = 0; k < 24; ++k) { SGB(0x8, 1); SGB(0x100, 1); }
        }
        sched0();
        if (rr_ == PUBAT) { if (early) publish(); STAMP(7); }
        if (rr_ == 2) { asm volatile("" ::: "memory"); prog_set(L, w, 20u); }
        sched0();
      }
    }
    STAMP(8); STAMP5(1);
    LAS char* xo_ = L + L_KIMG + cl * 2 * CS + rh_ * 4096;
    LAS char* xi_ = L + L_KIMG + cl * 2 * CS + (rh_ ^ 1) * 4096;
    const unsigned pa_p = lbase + L_PROG + (w ^ 1) * 4;
    const unsigned yo = (unsigned)((((rh_ * 16 + (i16 >> 2)) * 8 + (b >> 5)) * 64 + ((2 * b + cl) & 63)) * 256 + g * 64 + (i16 & 3) * 16);
    auto finish = [&](auto keep) {
      constexpr int K = decltype(keep)::value;
      spin(pa_p, 20u);
#pragma unroll
      for (int t = 0; t < 4; ++t) *(LAS f32x4*)(xo_ + t * 1024 + lane * 16) = y[(K ^ 4) + t];
      asm volatile("" ::: "memory");
      prog_set(L, w, 21u);
      spin(pa_p, 21u);
      f32x4 yp[4];
#pragma unroll
      for (int t = 0; t < 4; ++t) yp[t] = *(LAS f32x4*)(xi_ + t * 1024 + lane * 16);
#pragma unroll
      for (int t = 0; t < 4; ++t) {
        const f32x4 o = yp[t];
        const v4u v = {pk2s(y[K + t][0] + o[0], y[K + t][1] + o[1]), tag, pk2s(y[K + t][2] + o[2], y[K + t][3] + o[3]), tag};
        st16c(ws + WS_YT + t * (4 * 8 * 64 * 256), yo, v);
      }
    };
    if (rh_) finish(IC<4>{}); else finish(IC<0>{});
    if (EPRIO) __builtin_amdgcn_s_setprio(0);
    STAMP(9); STAMP5(2);
    spin_slow(lbase + L_PROG + (4 + w) * 4, 14u);
    STAMP5(3);
    unsigned ntry = 0;
    v4u a0, a1, a2, a3;
    {
      const char* yb = ws + WS_YT + (m2 * 8 + n2) * 16384 + hw * 4096;
#pragma unroll 1
      for (unsigned sp = 0; sp < SPIN_CAP; ++sp) {
        ld16c(a0, yb, lane * 16); ld16c(a1, yb + 1024, lane * 16); ld16c(a2, yb + 2048, lane * 16); ld16c(a3, yb + 3072, lane * 16);
        waitv<0>(); PIN(a0); PIN(a1); PIN(a2); PIN(a3);
        if (__all(a0.y == tag && a0.w == tag && a1.y == tag && a1.w == tag && a2.y == tag && a2.w == tag && a3.y == tag && a3.w == tag)) break;
        __builtin_amdgcn_s_sleep(YSL); ++ntry;
      }
    }
    STAMP5(4);
    {
      LAS char* p = xi_ + g * 128 + ((i16 & 3) * 16 + (i16 >> 2) * 4) * 2;
      *(LAS v2u*)p = v2u{a0.x, a0.z}; *(LAS v2u*)(p + 512) = v2u{a1.x, a1.z}; *(LAS v2u*)(p + 1024) = v2u{a2.x, a2.z}; *(LAS v2u*)(p + 1536) = v2u{a3.x, a3.z};
    }
    {
      v4u av[2], xv[2];
#pragma unroll
      for (int it = 0; it < 2; ++it) {
        const int c8 = hw * 2 + it;
        LAS char* p = xi_ + (it * 8 + (i16 >> 2)) * 128 + (g * 16 + (i16 & 3) * 4) * 2;
        av[it] = trd2(p, p + 512);
        xv[it] = *(LAS v4u*)(L + L_BASE + lane * 128 + ((c8 ^ (lane & 7)) * 16));
      }
#pragma unroll
      for (int it = 0; it < 2; ++it) {
        v4u o;
#pragma unroll
        for (int e = 0; e < 4; ++e) o[e] = pk2s(bflo(xv[it][e]) + bflo(av[it][e]), bfhi(xv[it][e]) + bfhi(av[it][e]));
        *(LAS v4u*)(L + L_BASE + lane * 128 + (((hw * 2 + it) ^ (lane & 7)) * 16)) = o;
      }
    }
    STAMP(10);
    asm volatile(".Lfk_e2:");
  } else {
    const unsigned pa_w = paddr(lbase, lane, 6, 1);
    const int kq = hw, r = lane & 31, h = lane >> 5;
    LAS char* W = L + kq * 2048;
    const int fb = min(22 * kq, 64);
    const int wa0 = (4 * h + (i16 >> 2)) * 128 + (i16 >> 3) * 64 + (g & 1) * 32 + (lane & 3) * 8;
    const int wa1 = wa0 ^ 64;
    const char* wds = (const char*)a.wd + (size_t)fb * 16384 + n2 * 128;
    int wi = rot2 + 5;
    wi = wi >= 22 ? wi - 22 : wi;
    auto wd_steps = [&](int n, int pos) {
#pragma unroll 1
      for (int i = 0; i < n; ++i) {
        dma2(wds + (size_t)wi * 16384, vd0, vd0 + 7168, lbase + pos + kq * 2048);
        wi = wi == 21 ? 0 : wi + 1;
        pos += 8192;
        waitv<WWIN>();
        if (PACEW) __builtin_amdgcn_s_sleep(PACEW);
      }
    };
    if (w >= 6) {
      const int wk = w - 6, wt = tid - 384;
      v4u rr[8];
      unsigned epoch_w, dsk_v;
      bar();
      {
        const char* own = (const char*)a.x + (size_t)(t0 + 4 * wk) * 1024 + (jl & 1) * 512;
        const char* oth = (const char*)a.x + (size_t)(t0 + 4 * wk) * 1024 + 512 - (jl & 1) * 512;
        const unsigned vo = g * 1024 + i16 * 16, vo2 = vo + 256, vo3 = vo + 8192, vo4 = vo + 8448;
        if (LNO && j < 15) spin_slow(lbase + L_PROG + 36, 1u);
        ld16(rr[0], own, vo); ld16(rr[1], own, vo2); ld16(rr[2], oth, vo); ld16(rr[3], oth, vo2);
        ld16(rr[4], own, vo3); ld16(rr[5], own, vo4); ld16(rr[6], oth, vo3); ld16(rr[7], oth, vo4);
        ld4c(epoch_w, ws + WS_EPOCH, 0u);
        ld2(dsk_v, a.dsk, (unsigned)(2 * b + wk) * 2);
      }
      waitv<0>();
#pragma unroll
      for (int e = 0; e < 8; ++e) PIN(rr[e]);
      PIN(epoch_w); PIN(dsk_v);
      const unsigned epoch = __builtin_amdgcn_readfirstlane(epoch_w);
      const unsigned tag = epoch * 0x9E3779B1u + 0x7F4A7C15u;
      prog_set(L, 8, tag);
      STAMP(1);
      v4u on[4];
      {
        float inv[2], nb[2];
#pragma unroll
        for (int q = 0; q < 2; ++q) {
          float s0 = 0.f, s1 = 0.f, q0 = 0.f, q1 = 0.f;
#pragma unroll
          for (int i = 0; i < 4; ++i) {
            s0 = dot2(rr[4 * q][i], 0x3F803F80u, s0); q0 = dot2(rr[4 * q][i], rr[4 * q][i], q0);
            s1 = dot2(rr[4 * q + 1][i], 0x3F803F80u, s1); q1 = dot2(rr[4 * q + 1][i], rr[4 * q + 1][i], q1);
            s0 = dot2(rr[4 * q + 2][i], 0x3F803F80u, s0); q0 = dot2(rr[4 * q + 2][i], rr[4 * q + 2][i], q0);
            s1 = dot2(rr[4 * q + 3][i], 0x3F803F80u, s1); q1 = dot2(rr[4 * q + 3][i], rr[4 * q + 3][i], q1);
          }
          const float mean = rsum16(cut(s0 + s1)) * (1.f / DM);
          const float var = fmaxf(rsum16(cut(q0 + q1)) * (1.f / DM) - mean * mean, 0.f);
          const float mu = rbf(mean), sd = rbf(__builtin_amdgcn_sqrtf(rbf(rbf(var) + 1e-5f)));
          inv[q] = __builtin_amdgcn_rcpf(sd); nb[q] = -mu * inv[q];
          if (i16 == 0 && !(jl & 1)) st8c(ws + WS_STAT, (unsigned)(t0 + 4 * wk + g + 8 * q) * 8, v2u{pk2(mu, sd), tag});
        }
#pragma unroll
        for (int k = 0; k < 2; ++k) {
          unsigned o[2][4];
#pragma unroll
          for (int q = 0; q < 2; ++q)
#pragma unroll
            for (int i = 0; i < 4; ++i) o[q][i] = pk2s(bflo(rr[4 * q + k][i]) * inv[q] + nb[q], bfhi(rr[4 * q + k][i]) * inv[q] + nb[q]);
          on[2 * k] = v4u{o[0][0], o[1][0], o[0][1], o[1][1]};
          on[2 * k + 1] = v4u{o[0][2], o[1][2], o[0][3], o[1][3]};
          PIN(on[2 * k]); PIN(on[2 * k + 1]);
        }
      }
      if (ABLW & 4) spin_slow(lbase + L_PROG + 36, j < 15 ? 2u : 0u);
      spin_slow(pa_c, 1u);
      f32x4 pt[2][4];
      {
        LAS char* p = L + L_UIMG + (4 * wk + g) * LNS + i16 * 32;
        *(LAS v4u*)p = on[0]; *(LAS v4u*)(p + 16) = on[1]; *(LAS v4u*)(p + 512) = on[2]; *(LAS v4u*)(p + 528) = on[3];
#pragma unroll
        for (int g4 = 0; g4 < 4; ++g4) { pt[0][g4] = *(LAS f32x4*)(L + part_off(2 * wk) + g4 * 1024 + lane * 16); pt[1][g4] = *(LAS f32x4*)(L + part_off(2 * wk + 1) + g4 * 1024 + lane * 16); }
      }
      asm volatile("" ::: "memory");
      prog_set(L, w, 1u);
      spin(pa_w, 1u);
      {
        LAS char* xp = L + L_UIMG + (i16 >> 2) * LNS + (wk * 64 + g * 4 + (lane & 3)) * 8;
        const unsigned xto = (unsigned)(((jl & 1) * 128 + wk * 64 + g * 4 + (i16 >> 2)) * 8192 + (t0 >> 4) * 64 + (lane & 3) * 16);
        v4u xv[4];
#pragma unroll
        for (int it = 0; it < 4; ++it) xv[it] = trd2(xp + it * 128, xp + it * 128 + 4 * LNS);
#pragma unroll
        for (int it = 0; it < 4; ++it) if (!(ABLW & 1)) st16c(ws + WS_XT + it * 16 * 8192, xto, xv[it]); else PIN(xv[it]);
      }
      {
        float v[16];
#pragma unroll
        for (int e = 0; e < 16; ++e) v[e] = cut(pt[0][e >> 2][e & 3] + pt[1][e >> 2][e & 3]);
        if (lane == 0) v[0] += bf2f(dsk_v);
        const int B = lane & 31, hh = lane >> 5;
        LAS char* k0 = L + L_KIMG + wk * 2 * CS + (255 - B) * 16;
        LAS char* ka = k0 - hh * 512;
        *(LAS v4u*)ka = v4u{pk2(v[7], v[6]), pk2(v[5], v[4]), pk2(v[3], v[2]), pk2(v[1], v[0])};
        *(LAS v4u*)(ka + CS) = v4u{pk2(v[8], v[7]), pk2(v[6], v[5]), pk2(v[4], v[3]), pk2(v[2], v[1])};
        const unsigned p00 = pk2(v[9], v[9]), p44 = pk2(v[10], v[10]), p88 = pk2(v[11], v[11]);
        *(LAS v4u*)(ka - 1024) = v4u{p88, p44, pk2(v[10], v[9]), p00};
        *(LAS v4u*)(ka - 1024 + CS) = v4u{p88, pk2(v[11], v[10]), p44, p00};
        LAS char* kc = k0 - 2048 - hh * 1024;
#pragma unroll
        for (int t = 0; t < 2; ++t) {
          const float lo = v[12 + 2 * t], hi = v[13 + 2 * t], md = (B & 1) ? hi : lo;
          const unsigned ll = pk2(lo, lo), hh2 = pk2(hi, hi);
          *(LAS v4u*)(kc - t * 512) = v4u{hh2, pk2(hi, md), ll, ll};
          *(LAS v4u*)(kc - t * 512 + CS) = v4u{hh2, hh2, pk2(md, lo), ll};
        }
        if (lane < 4) {
          *(LAS v4u*)(L + L_KIMG + wk * 2 * CS + 4096 + lane * 16) = v4u{0u, 0u, 0u, 0u};
          *(LAS v4u*)(L + L_KIMG + wk * 2 * CS + CS + 4096 + lane * 16) = v4u{lane == 0 ? pk2(v[0], 0.f) : 0u, 0u, 0u, 0u};
        }
      }
      waitv<0>();
      prog_done(L, w, 2u);
      if (__all(peek(pa_w) >= 2u)) { if (lane == 0) st4c(ws + WS_F1, (unsigned)b * 4, tag); }
      if (WDPF && wk == 1) { const char* p = (const char*)a.wd + min(b * 5504, FF * 1024 - 6144); stouch16<128>(p); stouch16<128>(p + 2048); stouch16<128>(p + 4096); }
      if (wk == 0 && j < 15) {
#pragma unroll 1
        for (unsigned sp = 0; sp < SPIN_CAP; ++sp) {
          ld16c(rr[4], ws + WS_STAT + m * 1024, lane * 16); waitv<0>(); PIN(rr[4]);
          if (__all(rr[4].y == tag && rr[4].w == tag)) break;
          __builtin_amdgcn_s_sleep(8);
        }
        *(LAS v2u*)(L + L_STAT + lane * 8) = v2u{rr[4].x, rr[4].z};
        asm volatile("" ::: "memory");
        prog_set(L, w, 3u);
        asm volatile(".Lfk_e3:");
      }
      STAMP(2);
      if (IDLEW && j == 15) { for (int i = 0; i < IDLEW; ++i) __builtin_amdgcn_s_sleep(16); }
      if (j < 15) spin_slow(lbase + L_PROG + 16, XTS);
      STAMP(3);
      v4u pfl;
#pragma unroll 1
      for (unsigned sp = 0; sp < SPIN_CAP; ++sp) {
        ld16c(pfl, ws + WS_F1, lane * 16); waitv<0>(); PIN(pfl);
        if (__all(pfl.x == tag && pfl.y == tag && pfl.z == tag && pfl.w == tag)) break;
        __builtin_amdgcn_s_sleep(8);
      }
      STAMP(4);
      if (j < 15) spin(lbase + L_PROG + 36, 2u);
      v4u u0, u1, u2, u3;
      const char* xb = ws + WS_XT + b * 8192;
      ld16c(u0, xb, wt * 16); ld16c(u1, xb + 2048, wt * 16); ld16c(u2, xb + 4096, wt * 16); ld16c(u3, xb + 6144, wt * 16);
      {
        const unsigned vo = (unsigned)((lane >> 3) * 1024 + (lane & 7) * 16);
        const char* xt = (const char*)a.x + (size_t)(m2 * 64 + wk * 32) * 1024 + n2 * 128;
        ld16(rr[0], xt, vo); ld16(rr[1], xt + 8192, vo); ld16(rr[2], xt + 16384, vo); ld16(rr[3], xt + 24576, vo);
      }
      if (b == 0 && wt == 0) st4c(ws + WS_EPOCH, 0u, epoch + 1u);
      LAS char* up = L + L_UIMG + (wt & 1) * US + 512 + ((wt >> 2) * 16 + ((wt >> 1) & 1) * 8) * 2;
      if (lane < 32) *(LAS v4u*)(L + L_UIMG + wk * US + lane * 16) = v4u{0u, 0u, 0u, 0u};
      waitv<0>(); PIN(u0); PIN(u1); PIN(u2); PIN(u3);
      *(LAS v4u*)up = u0; *(LAS v4u*)(up + 1024) = u1; *(LAS v4u*)(up + 2048) = u2; *(LAS v4u*)(up + 3072) = u3;
#pragma unroll
      for (int e = 0; e < 4; ++e) { PIN(rr[e]); *(LAS v4u*)(L + L_BASE + wk * 4096 + e * 1024 + (lane >> 3) * 128 + (((lane & 7) ^ (lane >> 3)) * 16)) = rr[e]; }
      asm volatile("" ::: "memory");
      prog_set(L, w, 5u);
      asm volatile(".Lfk_e1:");
      STAMP(5);
    } else {
      __builtin_amdgcn_s_setprio(LPRIO);
      unsigned vx[8], vw[12];
      {
        const int r = lane >> 3, cp = lane & 7;
#pragma unroll
        for (int i = 0; i < 12; ++i) { const int n = (i % 3) * 64 + lane; vw[i] = ((i / 3) * 16 + n / 12) * (FF * 2) + (n % 12) * 16 - (i & 3) * 1024; PIN(vw[i]); }
#pragma unroll
        for (int i = 0; i < 8; ++i) { vx[i] = r * 1024 + ((cp ^ ((i & 1) * 4 + g)) * 16) + i * 8192 - (i & 3) * 1024; PIN(vx[i]); }
      }
      bar();
      const int rot1 = ROT1 == 1 ? (m + 2 * j) & 7 : ROT1 == 2 ? (2 * (m + j)) & 7 : ROT1 == 3 ? (4 * (m + j)) & 7 : ROT1 == 4 ? (m + 2 * j) & 3 : 0;
      const char* xs0 = (const char*)a.x + (size_t)(m * 128) * 1024;
      const char* gs0 = (const char*)a.wg + f0 * 2;
      const char* us0 = (const char*)a.wu + f0 * 2;
      auto stage = [&](auto pace, int s, int sl) {
        constexpr int P = decltype(pace)::value;
        const unsigned lb = lbase + sl * RS;
        const int ks = (s + rot1) & 7;
        dma12<P>(gs0 + (size_t)ks * (64 * FF * 2), vw, lb + WGO);
        dma8<P>(xs0 + ks * 128, vx, lb);
        dma12<P>(us0 + (size_t)ks * (64 * FF * 2), vw, lb + WUO);
        dma8<P>(xs0 + ks * 128 + 65536, vx, lb + 8192);
      };
      auto burst = [&]() {
#pragma unroll
        for (int k = 0; k < 6; ++k) touch(a.wd, (unsigned)(slot * 344 + min(k * 58 + min(lane, 57), 343)) * 128, lbase + L_CSUM + 3072);
      };
      if (WDB && j == 15 && hw) { spin_slow(lbase + L_PROG + 24, 2u); burst(); }
      if (j < 15) {
        STAMP3(0);
        if (hw) spin(lbase + L_PROG + 36, 1u);
        stage(IC<0>{}, hw, hw);
        if (!hw) put1(lbase + L_PROG + 36, 1u);
        STAMP3(1);
        waitv<0>();
        prog_set(L, w, hw + 1);
        STAMP3(2);
        int sl = hw ? 0 : 2;
#pragma unroll 1
        for (int it = 1; it < 4; ++it) {
          const int s = 2 * it + hw;
          if (s >= 3) { if (LSL) spin_slow(pa_c, s - 1); else spin(pa_c, s - 1); }
          STAMP3(3 * it);
          if (FASTS && s < FASTS) stage(IC<P2>{}, s, sl); else stage(IC<PACE>{}, s, sl);
          STAMP3(3 * it + 1);
          sl = sl == 0 ? 2 : sl - 1;
          if (s == 7) put1(lbase + L_PROG + 36, 2u);
          if (WDB && s == 7) { if (BDEL) __builtin_amdgcn_s_sleep(BDEL); burst(); waitv<6>(); } else waitv<0>();
          prog_set(L, w, s + 1);
          STAMP3(3 * it + 2);
        }
      }
      STAMP3(12);
      if (!hw) {
        unsigned vd[8];
#pragma unroll
        for (int i = 0; i < 8; ++i) vd[i] = vd0 + (unsigned)(min(22 * (i >> 1), 64) * 16384 + (i & 1) * 8192 - (i & 3) * 1024);
        if (j < 15 && !hw) { spin(lbase + L_PROG + 36, 2u); __builtin_amdgcn_s_sleep(BDEL + ADEL); }
        spin(pa_c, 7u);
        STAMP(3);
        int ii = rot2 + 5 * hw;
        if (HOLE && !hw) {
          const int i6 = rot2 + 5 >= 22 ? rot2 - 17 : rot2 + 5;
          touch((const char*)a.wd + (size_t)i6 * 16384 + n2 * 128, (unsigned)(min(22 * g, 64) * 16 + i16) * 1024, lbase + L_CSUM + 3072);
        }
#pragma unroll 1
        for (int i = 0; i < 5; ++i) {
          ii = ii >= 22 ? ii - 22 : ii;
          dma8<PW8>((const char*)a.wd + (size_t)ii * 16384 + n2 * 128, vd, lbase + (hw ? 0 : 2 * RS) + i * 8192);
          ++ii;
        }
        STAMP(4);
        waitv<0>();
        prog_set(L, w, 9u + hw);
      }
      __builtin_amdgcn_s_setprio(0);
      STAMP(5);
    }
    LAS char* wp[3][2];
#pragma unroll
    for (int k = 0; k < 3; ++k) { wp[k][0] = W + wd_pos(5 * k) + wa0; wp[k][1] = W + wd_pos(5 * k) + wa1; PIN(wp[k][0]); PIN(wp[k][1]); }
    auto wfrag = [&](v4u* d, int i) {
      d[0] = trd2(wp[i / 5][0] + (i % 5) * 8192, wp[i / 5][0] + (i % 5) * 8192 + 1024); d[1] = trd2(wp[i / 5][1] + (i % 5) * 8192, wp[i / 5][1] + (i % 5) * 8192 + 1024);
    };
    LAS char* pw_[8];
#pragma unroll
    for (int k = 0; k < 8; ++k) { pw_[k] = W + (r >> 3) * 8192 + (r & 7) * 256 + (((2 * k + h) ^ (r & 15)) * 16); PIN(pw_[k]); }
    spin_slow(lbase + L_PROG + 24, 1u);
    unsigned tag = __builtin_amdgcn_readfirstlane(peek(lbase + L_PROG + 32));
    unsigned fo = (unsigned)min((22 * kq) / 3 + lane, (22 * kq + 21) / 3) * 128;
    const char* fbase = ws + WS_F2 + m2 * 4096;
    PIN(fo); asm volatile("" : "+s"(tag), "+s"(fbase));
    if (GPRIO) __builtin_amdgcn_s_setprio(GPRIO);
    spin_slow(lbase + L_PROG + 16, 9u);
    STAMP(6);
    v4u we[7][2];
#pragma unroll
    for (int i = 0; i < 5; ++i) wfrag(we[i], i);
    spin(pa_c, P9AT);
    wd_steps(5, 0);
    spin(pa_c, W3AT);
    STAMP(7);
    wd_steps(5, RS);
#pragma unroll
    for (int i = 5; i < 7; ++i) wfrag(we[i], i);
    wd_steps(5, 2 * RS);
#pragma unroll
    for (int i = 0; i < 7; ++i) { PIN(we[i][0]); PIN(we[i][1]); }
    waitl();
    wd_steps(2, 0);
    STAMP(8);
    const __amdgpu_buffer_rsrc_t rh = __builtin_amdgcn_make_buffer_rsrc((void*)(ws + WS_H), 0, H_BYTES, 0x00020000);
    poll_eq(fbase, fo, tag);
    if (GPRIO) __builtin_amdgcn_s_setprio(0);
    STAMP(9); STAMP5(0);
    constexpr int HW = 8;
    v4u hf[HW][2];
    auto ldh = [&](int i) {
      int ii = i + rot2; ii = ii >= 22 ? ii - 22 : ii;
      const int F16 = fb + ii;
      const int so = ((m2 * 2 * 86 + F16) * 1024) | (((F16 - 22 * kq) >> 31) & 0x40000000);
      hf[i % HW][0] = __builtin_amdgcn_raw_buffer_load_b128(rh, lane * 16, so, 16);
      hf[i % HW][1] = __builtin_amdgcn_raw_buffer_load_b128(rh, lane * 16, so + 86 * 1024, 16);
    };
#pragma unroll
    for (int i = 0; i < HW0; ++i) ldh(i);
    sched0();
    STAMP5(1);
    f32x16 o2[2][2];
#pragma unroll
    for (int i = 0; i < 4; ++i) o2[i >> 1][i & 1] = f32x16{};
    v4u wn[2][2];
    constexpr int NT = 22 - TAILN;
    int nld = HW0;
#pragma unroll
    for (int i = 0; i < NT; ++i) {
      if (i + 1 >= 7 && i + 1 < NT) {
        wfrag(wn[(i + 1) & 1], i + 1 < 15 ? i + 1 : i + 1 - 15);
      }
      if (i >= NT - TAILN) {
        const int k = i - (NT - TAILN);
        wfrag(we[k], NT + k - 15);
      }
      const v4u w0 = i < 7 ? we[i < 7 ? i : 0][0] : wn[i & 1][0], w1 = i < 7 ? we[i < 7 ? i : 0][1] : wn[i & 1][1];
      const v4u h0 = hf[i % HW][0], h1 = hf[i % HW][1];
      o2[0][0] = mfma32(w0, h0, o2[0][0]); o2[1][0] = mfma32(w1, h0, o2[1][0]);
      o2[0][1] = mfma32(w0, h1, o2[0][1]); o2[1][1] = mfma32(w1, h1, o2[1][1]);
#pragma unroll
      for (int k = 0; k < 2; ++k) if (nld < 22 && nld < i + HW) { ldh(nld); ++nld; }
      if (i == YAT) prog_set(L, w, 14u);
      SGB(0x8, 1); SGB(0x100, 2); SGB(0x20, 1); SGB(0x8, 1); SGB(0x100, 2); SGB(0x20, 1); SGB(0x8, 1); SGB(0x100, 2); SGB(0x20, 1); SGB(0x8, 1); SGB(0x100, 2); SGB(0x20, 1);
      sched0();
      if ((i & 1) == 0) { STAMP5(2 + i / 2); sched0(); }
    }
    asm volatile("" ::: "memory");
    auto wr = [&](int cc, int tt) {
#pragma unroll
      for (int g4 = 0; g4 < 4; ++g4)
        *(LAS f32x4*)(pw_[cc * 4 + g4] + tt * RS) = f32x4{o2[cc][tt][4 * g4], o2[cc][tt][4 * g4 + 1], o2[cc][tt][4 * g4 + 2], o2[cc][tt][4 * g4 + 3]};
    };
#pragma unroll
    for (int q = 0; q < 4; ++q) {
      const int cc = q & 1, tt = q >> 1;
#pragma unroll
      for (int k = 0; k < TAILN; ++k) o2[cc][tt] = mfma32(we[k][cc], hf[(NT + k) % HW][tt], o2[cc][tt]);
      if (q > 0) { wr((q - 1) & 1, (q - 1) >> 1); SGB(0x8, TW0); SGB(0x200, 1); SGB(0x8, 1); SGB(0x200, 1); SGB(0x8, 1); SGB(0x200, 1); SGB(0x8, 1); SGB(0x200, 1); SGB(0x8, 2); }
      sched0();
    }
    STAMP(10); STAMP5(12);
    wr(1, 1);
  }
  STAMP5(13);
  {
    const int tok = tid >> 3, c8 = tid & 7;
    LAS char* P = L + (tok >> 5) * RS + ((tok >> 3) & 3) * 8192 + (tok & 7) * 256;
    LAS char* p0 = P + (((2 * c8) ^ (tok & 15)) * 16);
    LAS char* p1 = P + (((2 * c8 + 1) ^ (tok & 15)) * 16);
    LAS char* pb = L + L_BASE + tok * 128 + ((c8 ^ (tok & 7)) * 16);
    unsigned oo = tok * 1024 + c8 * 16;
    char* ob = (char*)a.out + (size_t)(m2 * 64) * 1024 + n2 * 128;
    PIN(p0); PIN(p1); PIN(pb); PIN(oo); asm volatile("" : "+s"(ob));
    bar();
    STAMP5(14);
    asm volatile("" :: "s"(ka_touch));
    STAMP(11);
    f32x4 q0[4], q1[4];
#pragma unroll
    for (int k = 0; k < 4; ++k) { q0[k] = *(LAS f32x4*)(p0 + k * 2048); q1[k] = *(LAS f32x4*)(p1 + k * 2048); }
    const v4u bv = *(LAS v4u*)pb;
    sched0();
    const f32x4 s0 = (q0[0] + q0[1]) + (q0[2] + q0[3]), s1 = (q1[0] + q1[1]) + (q1[2] + q1[3]);
    const unsigned f0_ = pk2(s0[0], s0[1]), f1_ = pk2(s0[2], s0[3]), f2_ = pk2(s1[0], s1[1]), f3_ = pk2(s1[2], s1[3]);
    const v4u o = {pk2(bflo(bv.x) + bflo(f0_), bfhi(bv.x) + bfhi(f0_)), pk2(bflo(bv.y) + bflo(f1_), bfhi(bv.y) + bfhi(f1_)),
                   pk2(bflo(bv.z) + bflo(f2_), bfhi(bv.z) + bfhi(f2_)), pk2(bflo(bv.w) + bflo(f3_), bfhi(bv.w) + bfhi(f3_))};
    asm volatile("global_store_dwordx4 %0, %1, %2 sc0 sc1\n\ts_nop 1" :: "v"(oo), "v"(o), "s"(ob) : "memory");
  }
  asm volatile(".Lfk_end:");
}

extern "C" void kernel_launch(void* const* d_in, const int* in_sizes, int n_in, void* d_out, int out_size, void* d_ws, size_t ws_size,
                              hipStream_t stream) {
  Args a;
  a.x = (const u16*)d_in[0]; a.are = (const u16*)d_in[1]; a.aim = (const u16*)d_in[2]; a.bre = (const u16*)d_in[3]; a.bim = (const u16*)d_in[4];
  a.cre = (const u16*)d_in[5]; a.cim = (const u16*)d_in[6]; a.logdt = (const u16*)d_in[7]; a.dsk = (const u16*)d_in[8];
  a.wg = (const u16*)d_in[10]; a.wu = (const u16*)d_in[11]; a.wd = (const u16*)d_in[12];
  a.out = (u16*)d_out; a.ws = (char*)d_ws;
  (void)hipFuncSetAttribute((const void*)fused_kernel, hipFuncAttributeMaxDynamicSharedMemorySize, L_TOTAL);
  fused_kernel<<<256, 512, L_TOTAL, stream>>>(a);
}
```

```cpp
#include <hip/hip_runtime.h>
#include <hip/hip_bf16.h>

typedef unsigned short u16;
typedef unsigned v4u __attribute__((ext_vector_type(4)));
typedef unsigned v2u __attribute__((ext_vector_type(2)));
typedef short s16x4 __attribute__((ext_vector_type(4)));
typedef __bf16 bf16x8 __attribute__((ext_vector_type(8)));
typedef __bf16 bf16x2 __attribute__((ext_vector_type(2)));
typedef float f32x2 __attribute__((ext_vector_type(2)));
typedef float f32x4 __attribute__((ext_vector_type(4)));
typedef float f32x16 __attribute__((ext_vector_type(16)));
#define LAS __attribute__((address_space(3)))
#define DEVI __device__ __forceinline__

constexpr int DM = 512, FF = 1376;
constexpr int WS_EPOCH = 0;
constexpr int WS_F1 = 4096;
constexpr int WS_F2 = 1 << 18;
constexpr int WS_STAT = 16384;
constexpr int WS_XT = 1 << 20;
constexpr int WS_YT = 3 << 20;
constexpr int WS_H = 7 << 20;
constexpr int H_BYTES = 64 * 86 * 1024;
constexpr int RS = 41024, WGO = 16384, WUO = 16384 + 12320;
constexpr int CS = 4160;
constexpr int L_KIMG = 3 * RS;
constexpr int L_UIMG = L_KIMG + 4 * CS;
constexpr int US = 4608;
constexpr int L_ZT = L_KIMG, ZTS = 10064;
constexpr int L_CSUM = L_KIMG + 36864;
constexpr int L_PROG = L_ZT + 4 * ZTS;
constexpr int L_TOTAL = L_PROG + 64;
static_assert(L_TOTAL <= 163840, "lds");
DEVI int part_off(int w) { return L_ZT + (w == 2 ? 25856 : w * ZTS); }
constexpr int LNS = 1088;
constexpr int L_STAT = L_KIMG + 34304;
constexpr int L_BASE = L_KIMG + 25856;
constexpr unsigned SPIN_CAP = 1u << 17;
constexpr int PACE = 3, CAP = 12;
constexpr int WWIN = 4;
constexpr int HW = 8, HW0 = 4, TAILN = 6;
constexpr int P_TAG = 32, P_MARK = 36, YAT = 12;
constexpr int wd_pos(int i) { return ((i / 5 + 2) % 3) * RS + (i % 5) * 8192; }

DEVI float bf2f(unsigned b) { return __builtin_bit_cast(float, b << 16); }
DEVI float bflo(unsigned w) { return __builtin_bit_cast(float, w << 16); }
DEVI float bfhi(unsigned w) { return __builtin_bit_cast(float, w & 0xffff0000u); }
DEVI unsigned pk2v(f32x2 v) { return __builtin_bit_cast(unsigned, __builtin_convertvector(v, bf16x2)); }
DEVI unsigned pk2(float a, float b) { return pk2v(f32x2{a, b}); }
DEVI float rbf(float a) { return bflo(pk2(a, a)); }
DEVI f32x16 mfma32(v4u a, v4u b, f32x16 c) { return __builtin_amdgcn_mfma_f32_32x32x16_bf16(__builtin_bit_cast(bf16x8, a), __builtin_bit_cast(bf16x8, b), c, 0, 0, 0); }
DEVI f32x4 mfma16(v4u a, v4u b, f32x4 c) { return __builtin_amdgcn_mfma_f32_16x16x32_bf16(__builtin_bit_cast(bf16x8, a), __builtin_bit_cast(bf16x8, b), c, 0, 0, 0); }
DEVI v2u trd(LAS char* p) { return __builtin_bit_cast(v2u, __builtin_amdgcn_ds_read_tr16_b64_v4i16((LAS s16x4*)p)); }
DEVI v4u trd2(LAS char* p0, LAS char* p1) { v2u a = trd(p0), b = trd(p1); return v4u{a.x, a.y, b.x, b.y}; }
template <int N> DEVI void waitv() { asm volatile("s_waitcnt vmcnt(%0)" :: "i"(N) : "memory"); }
DEVI void waitl() { asm volatile("" ::: "memory"); __builtin_amdgcn_s_waitcnt(0xC07F); asm volatile("" ::: "memory"); }
DEVI void bar() { waitl(); asm volatile("s_waitcnt lgkmcnt(0)\n\ts_barrier" ::: "memory"); }
DEVI void sched0() { __builtin_amdgcn_sched_barrier(0); }
#define SGB(mask, n) __builtin_amdgcn_sched_group_barrier(mask, n, 0)
DEVI void ld16(v4u& r, const void* sbase, unsigned voff) { asm volatile("global_load_dwordx4 %0, %1, %2" : "=v"(r) : "v"(voff), "s"(sbase) : "memory"); }
DEVI void ld16c(v4u& r, const void* sbase, unsigned voff) { asm volatile("global_load_dwordx4 %0, %1, %2 sc1" : "=v"(r) : "v"(voff), "s"(sbase) : "memory"); }
DEVI void ld4c(unsigned& r, const void* sbase, unsigned voff) { asm volatile("global_load_dword %0, %1, %2 sc1" : "=v"(r) : "v"(voff), "s"(sbase) : "memory"); }
DEVI void ld2(unsigned& r, const void* sbase, unsigned voff) { asm volatile("global_load_ushort %0, %1, %2" : "=v"(r) : "v"(voff), "s"(sbase) : "memory"); }
DEVI void st16c(void* sbase, unsigned voff, v4u v) { asm volatile("global_store_dwordx4 %0, %1, %2 sc1\n\ts_nop 1" :: "v"(voff), "v"(v), "s"(sbase) : "memory"); }
DEVI void st8c(void* sbase, unsigned voff, v2u v) { asm volatile("global_store_dwordx2 %0, %1, %2 sc1\n\ts_nop 1" :: "v"(voff), "v"(v), "s"(sbase) : "memory"); }
DEVI void st4c(void* sbase, unsigned voff, unsigned v) { asm volatile("global_store_dword %0, %1, %2 sc1\n\ts_nop 1" :: "v"(voff), "v"(v), "s"(sbase) : "memory"); }
DEVI void flag1(void* sbase, unsigned voff, unsigned v) {
  asm volatile("s_mov_b64 exec, 1\n\tglobal_store_dword %0, %1, %2 sc1\n\ts_mov_b64 exec, -1" :: "v"(voff), "v"(v), "s"(sbase) : "memory");
}
#define PIN(x) asm volatile("" : "+v"(x))
template <int C> DEVI float dppf(float v) { return __builtin_bit_cast(float, __builtin_amdgcn_update_dpp(0, __builtin_bit_cast(int, v), C, 0xf, 0xf, true)); }
DEVI float rsum16(float v) { v += dppf<0xB1>(v); v += dppf<0x4E>(v); v += dppf<0x141>(v); v += dppf<0x140>(v); return v; }
DEVI float cut(float x) { asm("" : "+v"(x)); return x; }
DEVI unsigned pk2s(float a, float b) { return pk2(cut(a), cut(b)); }
DEVI float dot2(unsigned a, unsigned b, float c) { return __builtin_amdgcn_fdot2_f32_bf16(__builtin_bit_cast(bf16x2, a), __builtin_bit_cast(bf16x2, b), c, false); }
DEVI void dma2(const void* sbase, unsigned v0, unsigned v1, unsigned ldsaddr) {
  asm volatile("s_mov_b32 m0, %3\n\ts_nop 0\n\tglobal_load_lds_dwordx4 %0, %2\n\tglobal_load_lds_dwordx4 %1, %2 offset:1024" :: "v"(v0), "v"(v1), "s"(sbase), "s"(ldsaddr) : "memory");
}
#define DMA4(a, b, c, d, sb, lb, off, p, q) \
  "s_waitcnt vmcnt(%" #q ")\n\ts_add_i32 m0, %" #lb ", " #off "\n\ts_nop %" #p "\n\tglobal_load_lds_dwordx4 %" #a ", %" #sb "\n\ts_nop %" #p "\n\tglobal_load_lds_dwordx4 %" #b ", %" #sb " offset:1024\n\t" \
  "s_nop %" #p "\n\tglobal_load_lds_dwordx4 %" #c ", %" #sb " offset:2048\n\ts_nop %" #p "\n\tglobal_load_lds_dwordx4 %" #d ", %" #sb " offset:3072\n\t"
template <int P> DEVI void dma8(const void* sbase, const unsigned* v, unsigned lb) {
  asm volatile(DMA4(0, 1, 2, 3, 8, 9, 0, 10, 11) DMA4(4, 5, 6, 7, 8, 9, 4096, 10, 11)
               :: "v"(v[0]), "v"(v[1]), "v"(v[2]), "v"(v[3]), "v"(v[4]), "v"(v[5]), "v"(v[6]), "v"(v[7]), "s"(sbase), "s"(lb), "i"(P), "i"(P ? CAP : 63) : "memory", "scc");
}
template <int P> DEVI void dma12(const void* sbase, const unsigned* v, unsigned lb) {
  asm volatile(DMA4(0, 1, 2, 3, 12, 13, 0, 14, 15) DMA4(4, 5, 6, 7, 12, 13, 4096, 14, 15) DMA4(8, 9, 10, 11, 12, 13, 8192, 14, 15)
               :: "v"(v[0]), "v"(v[1]), "v"(v[2]), "v"(v[3]), "v"(v[4]), "v"(v[5]), "v"(v[6]), "v"(v[7]), "v"(v[8]), "v"(v[9]), "v"(v[10]), "v"(v[11]),
                  "s"(sbase), "s"(lb), "i"(P), "i"(P ? CAP : 63) : "memory", "scc");
}
DEVI void touch(const void* sbase, unsigned voff, unsigned ldsdump) {
  asm volatile("s_mov_b32 m0, %2\n\ts_nop 0\n\tglobal_load_lds_dword %0, %1" :: "v"(voff), "s"(sbase), "s"(ldsdump) : "memory");
}
#define CMM(r, a, x) "v_pk_mul_f32 %" #r ", %" #a ", %" #x " op_sel:[1,1] op_sel_hi:[1,0]\n\t"
#define CMF(r, a, x) "v_pk_fma_f32 %" #r ", %" #a ", %" #x ", %" #r " op_sel_hi:[0,1,1] neg_lo:[0,0,1]\n\t"
DEVI void cm4(f32x2& r0, f32x2& r1, f32x2& r2, f32x2& r3, f32x2 a0, f32x2 x0, f32x2 a1, f32x2 x1, f32x2 a2, f32x2 x2, f32x2 a3, f32x2 x3) {
  asm(CMM(0, 4, 5) CMM(1, 6, 7) CMM(2, 8, 9) CMM(3, 10, 11) CMF(0, 4, 5) CMF(1, 6, 7) CMF(2, 8, 9) CMF(3, 10, 11)
      : "=&v"(r0), "=&v"(r1), "=&v"(r2), "=&v"(r3) : "v"(a0), "v"(x0), "v"(a1), "v"(x1), "v"(a2), "v"(x2), "v"(a3), "v"(x3));
}
DEVI void put1(unsigned addr, unsigned v) {
  asm volatile("s_mov_b64 exec, 1\n\tds_write_b32 %0, %1\n\ts_mov_b64 exec, -1" :: "v"(addr), "v"(v) : "memory");
}
DEVI void prog_set(LAS char* L, int w, unsigned v) { put1((unsigned)(size_t)L + L_PROG + w * 4, v); }
DEVI void prog_done(LAS char* L, int w, unsigned v) { waitl(); prog_set(L, w, v); }
DEVI unsigned paddr(unsigned lbase, int lane, int w0, int mask) { return lbase + L_PROG + (w0 + (lane & mask)) * 4; }
DEVI void spin(unsigned addr, unsigned n) {
  unsigned t, c = 1u << 22;
  asm volatile("1:\n\tds_read_b32 %0, %2\n\ts_sub_u32 %1, %1, 1\n\ts_waitcnt lgkmcnt(0)\n\tv_cmp_gt_u32 vcc, %3, %0\n\ts_cselect_b64 vcc, 0, vcc\n\ts_cbranch_vccnz 1b"
               : "=&v"(t), "+s"(c) : "v"(addr), "s"(n) : "memory", "vcc", "scc");
}
DEVI void spin_if(unsigned seen, unsigned addr, unsigned n) {
  unsigned t, c;
  asm volatile("s_cmp_ge_u32 %4, %3\n\ts_cbranch_scc1 2f\n\ts_mov_b32 %1, 0x400000\n"
               "1:\n\tds_read_b32 %0, %2\n\ts_sub_u32 %1, %1, 1\n\ts_waitcnt lgkmcnt(0)\n\tv_cmp_gt_u32 vcc, %3, %0\n\ts_cselect_b64 vcc, 0, vcc\n\ts_cbranch_vccnz 1b\n2:"
               : "=&v"(t), "=&s"(c) : "v"(addr), "s"(n), "s"(seen) : "memory", "vcc", "scc");
}
DEVI void spin_slow(unsigned addr, unsigned n) {
  unsigned t, c = 1u << 21;
  asm volatile("1:\n\tds_read_b32 %0, %2\n\ts_waitcnt lgkmcnt(0)\n\tv_cmp_gt_u32 vcc, %3, %0\n\ts_cbranch_vccz 2f\n\ts_sleep 2\n\ts_sub_u32 %1, %1, 1\n\ts_cbranch_scc0 1b\n2:"
               : "=&v"(t), "+s"(c) : "v"(addr), "s"(n) : "memory", "vcc", "scc");
}
DEVI void poll_eq(const void* sbase, unsigned voff, unsigned want) {
  unsigned t, c = SPIN_CAP;
  asm volatile("1:\n\tglobal_load_dword %0, %2, %3 sc1\n\ts_sub_u32 %1, %1, 1\n\ts_waitcnt vmcnt(0)\n\tv_cmp_ne_u32 vcc, %4, %0\n\ts_cselect_b64 vcc, 0, vcc\n\ts_cbranch_vccnz 1b"
               : "=&v"(t), "+s"(c) : "v"(voff), "s"(sbase), "s"(want) : "memory", "vcc", "scc");
}
DEVI unsigned peek(unsigned addr) { return *(volatile LAS unsigned*)(size_t)addr; }
template <int N> struct IC { static constexpr int value = N; };
struct Args {
  const u16 *x, *are, *aim, *bre, *bim, *cre, *cim, *logdt, *dsk, *wg, *wu, *wd;
  u16* out;
  char* ws;
};

__global__ void __launch_bounds__(512, 2) fused_kernel(Args a) {
  extern __shared__ __attribute__((aligned(16))) char smem[];
  LAS char* L = (LAS char*)smem;
  const unsigned lbase = (unsigned)(size_t)L;
  unsigned ka_touch;
  asm volatile("s_load_dword %0, %1, 0x0" : "=s"(ka_touch) : "s"(__builtin_amdgcn_kernarg_segment_ptr()));
  unsigned long long pc; unsigned sz;
  asm volatile("s_getpc_b64 %0\n.Lfk_start:\n\ts_mov_b32 %1, .Lfk_end-.Lfk_start\n\ts_max_u32 %1, %1, .Lfk_e1-.Lfk_start\n\ts_max_u32 %1, %1, .Lfk_e2-.Lfk_start\n\ts_max_u32 %1, %1, .Lfk_e3-.Lfk_start"
               : "=s"(pc), "=s"(sz) :: "scc");
  const int tid = threadIdx.x, lane = tid & 63;
  const int w = __builtin_amdgcn_readfirstlane(tid >> 6);
  const int hw = w & 3;
  const int b = blockIdx.x, xcd = b & 7, slot = b >> 3;
  unsigned codetouch = 0;
  {
    const unsigned off = (unsigned)((lane * 8 + (slot & 7)) * 128);
    if (w == 3 && off < sz) asm volatile("global_load_dword %0, %1, %2" : "=v"(codetouch) : "v"(off), "s"(pc) : "memory");
  }
  prog_set(L, w, 0u); prog_set(L, w + 8, 0u);
  const int tl = 30 * (xcd & 3) + slot;
  const int m = (xcd >> 2) * 8 + (tl & 7), j = slot < 30 ? tl >> 3 : 15;
  const int jl = (xcd & 3) * 4 + (slot & 3);
  const int f0 = min(96 * j, FF - 96);
  const int m2 = xcd * 4 + (slot >> 3), n2 = slot & 7;
  const int t0 = ((xcd >> 2) * 8 + (slot >> 2)) * 128 + (jl >> 1) * 16;
  const int i16 = lane & 15, g = lane >> 4;
  char* ws = a.ws;
  const int rot2 = ((n2 * 11) / 4 + ((slot >> 3) * 11) / 2 + xcd * 3) % 22;
  const unsigned vd0 = (lane >> 3) * 1024 + (((lane & 7) ^ ((g & 1) << 2)) * 16);
  const unsigned pa_c = paddr(lbase, lane, 0, 3);

  if (w < 4) {
    unsigned pq[7], epoch_v;
    {
      const unsigned po = (unsigned)(b * 256 + tid) * 2;
      ld2(pq[6], a.logdt, (unsigned)(2 * b + (w >> 1)) * 2);
      ld2(pq[0], a.are, po); ld2(pq[1], a.aim, po); ld2(pq[2], a.bre, po); ld2(pq[3], a.bim, po); ld2(pq[4], a.cre, po); ld2(pq[5], a.cim, po);
      ld4c(epoch_v, ws + WS_EPOCH, 0u);
    }
    bar();
    const int tg = w & 1, fh = w >> 1;
    const int r = lane & 31, h = lane >> 5;
    const unsigned one2 = ((i16 & 7) == 4 * (g & 1)) ? 0x3F803F80u : 0u;
    v4u sel = {one2, one2, one2, one2};
    int xo[4], wo[2];
#pragma unroll
    for (int p = 0; p < 4; ++p) xo[p] = (tg * 64 + r) * 128 + (((2 * (p ^ (2 * tg)) + h) ^ ((r >> 1) & 7)) * 16);
    wo[0] = WGO + (g & 1) * 12320 + (8 * h + (i16 >> 2)) * 192 + fh * 96 + (lane & 3) * 8;
    wo[1] = wo[0] + (1 - tg) * 6144;
    wo[0] += tg * 6144;
    int zrow;
    {
      const int e = (r & 3) + 4 * (r >> 3), hh = (r >> 2) & 1;
      const unsigned long long lo = hh ? 0x1019181818170909ull : 0x0706050403020100ull;
      const unsigned long long hi = hh ? 0x160F150E121B0B10ull : 0x140D130C111A0A08ull;
      zrow = (int)(((e < 8 ? lo : hi) >> (8 * (e & 7))) & 31);
    }
    int zc_ = L_ZT + w * ZTS + zrow * 272 + h * 16, zf_ = L_ZT + w * ZTS + r * 272 + h * 16;
    const unsigned pa_l = lbase + L_PROG + 16;
#pragma unroll
    for (int p = 0; p < 4; ++p) PIN(xo[p]);
    PIN(wo[0]); PIN(wo[1]); PIN(zc_); PIN(zf_); PIN(sel);
    waitv<0>();
#pragma unroll
    for (int e = 0; e < 7; ++e) PIN(pq[e]);
    PIN(codetouch); PIN(epoch_v);
    const unsigned tag = __builtin_amdgcn_readfirstlane(epoch_v) * 0x9E3779B1u + 0x7F4A7C15u;
    {
      const float dt = rbf(expf(bf2f(pq[6])));
      const float ar = (dt * bf2f(pq[0])) * 1.44269504088896341f;
      float ai = (dt * bf2f(pq[1])) * 0.15915494309189535f;
      ai -= __builtin_rintf(ai);
      const float br = dt * bf2f(pq[2]), bi = dt * bf2f(pq[3]), cr0 = bf2f(pq[4]), ci0 = bf2f(pq[5]);
      f32x2 C = {cr0 * br - ci0 * bi, -(cr0 * bi + ci0 * br)};
      const f32x2 zc = f32x2{__builtin_amdgcn_cosf(ai), -__builtin_amdgcn_sinf(ai)} * __builtin_amdgcn_exp2f(ar);
      const f32x2 u8 = f32x2{__builtin_amdgcn_cosf(ai * 8.f), __builtin_amdgcn_sinf(ai * 8.f)} * __builtin_amdgcn_exp2f(ar * 8.f);
      f32x2 b0 = f32x2{__builtin_amdgcn_cosf(ai * 128.f), __builtin_amdgcn_sinf(ai * 128.f)} * __builtin_amdgcn_exp2f(ar * 128.f);
      const f32x2 u8c = {u8.x, -u8.y}, b0c = {b0.x, -b0.y};
      LAS unsigned* zt = (LAS unsigned*)(L + L_ZT + w * ZTS + lane * 4);
      f32x2 vc, E, b1, u16_, D = C, ex = C;
      cm4(vc, E, b1, u16_, b0c, b0c, C, u8c, b0, u8, u8, u8);
      zt[0] = pk2v(C); zt[8 * 68] = pk2v(E);
      f32x2 c6 = C;
#pragma unroll
      for (int k = 1; k < 8; ++k) {
        if (k == 3 || k == 5) ex = C;
        if (k == 7) c6 = C;
        cm4(C, D, E, ex, C, zc, D, vc, E, vc, ex, vc);
        zt[k * 68] = pk2v(C); zt[(8 + k) * 68] = pk2v(D); zt[(15 + k) * 68] = pk2v(E);
        if (k == 3) zt[23 * 68] = pk2v(ex);
        if (k == 5) zt[24 * 68] = pk2v(ex);
        if (k == 6) zt[26 * 68] = pk2v(ex);
        if (k == 7) zt[27 * 68] = pk2v(ex);
      }
      f32x2 a0 = u16_, a1, b2, b3;
      cm4(c6, a1, b2, b3, c6, vc, u8, u16_, b0, u16_, b1, u16_);
      zt[25 * 68] = pk2v(c6);
      asm volatile("" ::: "memory");
      v4u fc[8], ff[8];
#pragma unroll
      for (int ks = 0; ks < 8; ++ks) fc[ks] = *(LAS v4u*)(L + zc_ + ks * 32);
      asm volatile("" ::: "memory");
      zt[0] = 0x00003F80u; zt[68] = pk2v(u8); zt[16 * 68] = pk2v(b0); zt[17 * 68] = pk2v(b1);
      zt[2 * 68] = pk2v(a0); zt[3 * 68] = pk2v(a1); zt[18 * 68] = pk2v(b2); zt[19 * 68] = pk2v(b3);
#pragma unroll
      for (int k = 2; k < 8; ++k) {
        cm4(a0, a1, b2, b3, a0, u16_, a1, u16_, b2, u16_, b3, u16_);
        zt[2 * k * 68] = pk2v(a0); zt[(2 * k + 1) * 68] = pk2v(a1); zt[(16 + 2 * k) * 68] = pk2v(b2); zt[(17 + 2 * k) * 68] = pk2v(b3);
      }
      asm volatile("" ::: "memory");
#pragma unroll
      for (int ks = 0; ks < 8; ++ks) ff[ks] = *(LAS v4u*)(L + zf_ + ks * 32);
      sched0();
      f32x16 kd = {};
#pragma unroll
      for (int ks = 0; ks < 8; ++ks) kd = mfma32(fc[ks], ff[ks], kd);
#pragma unroll
      for (int g4 = 0; g4 < 4; ++g4) *(LAS f32x4*)(L + part_off(w) + g4 * 1024 + lane * 16) = f32x4{kd[4 * g4], kd[4 * g4 + 1], kd[4 * g4 + 2], kd[4 * g4 + 3]};
      asm volatile("" ::: "memory");
      prog_set(L, w, 1u);
    }
    if (2 * j - fh < 28) {
      f32x16 acc[2][3];
#pragma unroll
      for (int i = 0; i < 2; ++i)
#pragma unroll
        for (int c = 0; c < 3; ++c) acc[i][c] = f32x16{};
      f32x4 cs[3] = {};
      v4u xf[2][2], wf[2][3];
      auto ldf = [&](int buf, int sl, int p) {
        LAS char* S = L + sl * RS;
        LAS char* Wp = S + wo[p >> 1] + (p & 1) * 3072;
        wf[buf][0] = trd2(Wp, Wp + 768);
        xf[buf][0] = *(LAS v4u*)(S + xo[p]);
        xf[buf][1] = *(LAS v4u*)(S + xo[p] + 4096);
        wf[buf][1] = trd2(Wp + 32, Wp + 800);
        wf[buf][2] = trd2(Wp + 64, Wp + 832);
      };
      auto mmc = [&](int buf, int c) { acc[0][c] = mfma32(wf[buf][c], xf[buf][0], acc[0][c]); acc[1][c] = mfma32(wf[buf][c], xf[buf][1], acc[1][c]); };
      auto mm = [&](int buf, bool dosel) {
        mmc(buf, 0); mmc(buf, 1); mmc(buf, 2);
        if (dosel) {
#pragma unroll
          for (int c = 0; c < 3; ++c) cs[c] = mfma16(sel, wf[buf][c], cs[c]);
        }
      };
      auto pattern = [&]() { SGB(0x8, 1); SGB(0x100, 2); SGB(0x8, 1); SGB(0x100, 2); SGB(0x8, 1); SGB(0x100, 2); SGB(0x8, 1); SGB(0x100, 3); SGB(0x8, 5); sched0(); };
      spin(pa_l, 1u);
      ldf(0, 0, 0);
      sched0();
      unsigned pw = lbase + L_PROG + w * 4;
      unsigned pl[2] = {pa_l, pa_l + 4};
      PIN(pw); PIN(pl[0]); PIN(pl[1]);
#pragma unroll
      for (int s = 0; s < 7; ++s) {
        const int sl = s % 3;
        ldf(1, sl, 1); mm(0, true); pattern();
        ldf(0, sl, 2);
        const unsigned full = peek(pl[(s + 1) & 1]);
        unsigned pv = s + 2;
        PIN(pv);
        mm(1, true); pattern();
        ldf(1, sl, 3);
        unsigned fs = __builtin_amdgcn_readfirstlane(full);
        mmc(0, 0); mmc(0, 1);
        SGB(0x8, 1); SGB(0x100, 2); SGB(0x2, 1); SGB(0x8, 1); SGB(0x100, 2); SGB(0x8, 1); SGB(0x100, 2); SGB(0x8, 1); SGB(0x100, 2);
        sched0();
        if (s == 6) {
#pragma unroll
          for (int c = 0; c < 3; ++c) *(LAS float*)(L + L_CSUM + w * 768 + c * 256 + lane * 4) = cs[c][0];
          asm volatile("" ::: "memory");
        }
        put1(pw, pv);
        sched0();
        mmc(0, 2);
        sched0();
        spin_if(fs, pl[(s + 1) & 1], s + 2);
        asm volatile("" ::: "memory");
        sched0();
        ldf(0, (s + 1) % 3, 0); mm(1, false); pattern();
      }
      const unsigned pq_ = peek(lbase + L_PROG + 24);
      ldf(1, 1, 1);
      const unsigned pp_ = peek(lbase + L_PROG + (w ^ 1) * 4);
      mmc(0, 0); mmc(0, 1);
      SGB(0x8, 1); SGB(0x100, 3); SGB(0x8, 1); SGB(0x100, 3); SGB(0x8, 1); SGB(0x100, 2); SGB(0x8, 1); SGB(0x100, 2); sched0();
      const unsigned ws_ = __builtin_amdgcn_readfirstlane(pq_);
      mmc(0, 2);
#pragma unroll
      for (int c = 0; c < 3; ++c) cs[c] = mfma16(sel, wf[0][c], cs[c]);
      sched0();
      spin_if(ws_, lbase + L_PROG + 24, 3u);
      sched0();
      const unsigned sv[2] = {*(LAS unsigned*)(L + L_STAT + (tg * 64 + r) * 4), *(LAS unsigned*)(L + L_STAT + (tg * 64 + 32 + r) * 4)};
      ldf(0, 1, 2);
      const unsigned ps_ = __builtin_amdgcn_readfirstlane(pp_);
      float c1[2], ir2[2];
      v4u xa[2], wa[3];
#pragma unroll
      for (int i = 0; i < 2; ++i) {
        const unsigned nm = h ? 0u : (sv[i] & 0xffffu) ^ 0x8000u;
        xa[i] = v4u{nm | (nm << 16), nm, 0u, 0u};
        const float sd = bfhi(sv[i]);
        c1[i] = cut(-1.44269504088896341f * __builtin_amdgcn_rcpf(sd)); ir2[i] = cut(sd * sd);
        asm("" : "+v"(xa[i]));
      }
      mm(1, true);
      SGB(0x8, 1); SGB(0x100, 3); SGB(0x2, 2); SGB(0x8, 1); SGB(0x100, 3); SGB(0x8, 1); SGB(0x100, 2); SGB(0x8, 1); SGB(0x100, 2); SGB(0x2, 3);
      SGB(0x8, 1); SGB(0x2, 5); SGB(0x8, 1); SGB(0x2, 5); SGB(0x8, 1); SGB(0x2, 2); SGB(0x8, 1); SGB(0x2, 2); SGB(0x8, 1); sched0();
      spin_if(ps_, lbase + L_PROG + (w ^ 1) * 4, 8u);
      asm volatile("" ::: "memory");
      sched0();
      ldf(1, 1, 3);
      float csp[3];
#pragma unroll
      for (int c = 0; c < 3; ++c) csp[c] = *(LAS float*)(L + L_CSUM + (w ^ 1) * 768 + c * 256 + lane * 4);
      mmc(0, 0); mmc(0, 1);
      SGB(0x8, 1); SGB(0x100, 3); SGB(0x8, 1); SGB(0x100, 3); SGB(0x8, 1); SGB(0x100, 3); SGB(0x8, 1); SGB(0x100, 2); sched0();
      __builtin_amdgcn_s_setprio(1);
      put1(pw, 10u);
      sched0();
      mmc(0, 2);
#pragma unroll
      for (int c = 0; c < 3; ++c) cs[c] = mfma16(sel, wf[0][c], cs[c]);
#pragma unroll
      for (int c = 0; c < 3; ++c) cs[c] = mfma16(sel, wf[1][c], cs[c]);
      sched0();
      mmc(1, 0);
      sched0();
#pragma unroll
      for (int c = 0; c < 3; ++c) {
        const float s = cut(cs[c][0] + csp[c]);
        const float hi = bfhi(__builtin_bit_cast(unsigned, s)), d1 = cut(s - hi), mid = bfhi(__builtin_bit_cast(unsigned, d1)), lo = cut(d1 - mid);
        wa[c] = v4u{__builtin_amdgcn_perm(__builtin_bit_cast(unsigned, d1), __builtin_bit_cast(unsigned, s), 0x07060302u), __builtin_bit_cast(unsigned, lo) >> 16, 0u, 0u};
      }
      mmc(1, 1); mmc(1, 2);
      SGB(0x2, 5); SGB(0x8, 1); SGB(0x2, 6); SGB(0x8, 1); SGB(0x2, 6); SGB(0x8, 1); SGB(0x2, 6); SGB(0x8, 1); sched0();
#pragma unroll
      for (int c = 0; c < 3; ++c) { acc[0][c] = mfma32(wa[c], xa[0], acc[0][c]); acc[1][c] = mfma32(wa[c], xa[1], acc[1][c]); }
      const __amdgpu_buffer_rsrc_t rhs = __builtin_amdgcn_make_buffer_rsrc((void*)(ws + WS_H), 0, H_BYTES, 0x00020000);
#pragma unroll
      for (int c = 0; c < 3; ++c) {
        const int F16 = f0 / 16 + fh * 3 + c;
#pragma unroll
        for (int i = 0; i < 2; ++i) {
          float hv[8];
#pragma unroll
          for (int e = 0; e < 8; ++e) {
            const float gv = acc[i][c][e], u = acc[i][c][e + 8];
            const float ex = __builtin_amdgcn_exp2f(gv * c1[i]);
            hv[e] = (gv * u) * __builtin_amdgcn_rcpf(ex * ir2[i] + ir2[i]);
          }
          const v4u o = {pk2(hv[0], hv[1]), pk2(hv[2], hv[3]), pk2(hv[4], hv[5]), pk2(hv[6], hv[7])};
          const int so = (((m * 4 + tg * 2 + i) * 86 + F16) * 1024) | (((F16 - 6 * j) >> 31) & 0x40000000);
          __builtin_amdgcn_raw_buffer_store_b128(o, rhs, lane * 16, so, 16);
          asm volatile("s_nop 1");
          sched0();
        }
      }
    }
    prog_set(L, w, 12u);
    const int cl = w >> 1, rh_ = w & 1;
    f32x4 y[8];
#pragma unroll
    for (int t = 0; t < 8; ++t) y[t] = f32x4{};
    {
      const int s0 = 2031 - i16 + 8 * g - rh_ * 128 - 96;
      LAS char* ka = L + L_KIMG + cl * 2 * CS + (s0 & 1) * CS + ((s0 + 1) >> 1) * 4;
      LAS char* ub = L + L_UIMG + cl * US + 512 + (16 * i16 - 16 + 8 * g - rh_ * 128 - 96) * 2;
      LAS char* kq_[4];
#pragma unroll
      for (int q = 0; q < 4; ++q) { kq_[q] = ka - (2 * q + 1) * 512; asm volatile("" : "+v"(kq_[q])); }
      v4u fa[2][8], fb[2][8];
      auto ldc = [&](int buf, int rr_) {
#pragma unroll
        for (int q = 0; q < 8; ++q) {
          LAS unsigned* p = (LAS unsigned*)(kq_[q >> 1] + ((q & 1) ? 0 : 512) + (3 - rr_) * 64);
          fa[buf][q] = v4u{p[0], p[1], p[2], p[3]};
          fb[buf][q] = *(LAS v4u*)(ub + q * 512 + (3 - rr_) * 64);
        }
      };
      auto publish = [&]() {
        waitv<0>();
        if (j < 15) flag1(ws + WS_F2, (unsigned)(((m * 2 + tg) * 16 + j) * 2 + fh) * 128, tag);
        prog_set(L, w, 13u);
      };
      const unsigned pa_w = paddr(lbase, lane, 6, 1);
      bool early = true;
      if (__any(peek(pa_w) < 5u)) { publish(); early = false; spin_slow(pa_w, 5u); }
      ldc(0, 0);
      sched0();
      if (early) publish();
      sched0();
#pragma unroll
      for (int rr_ = 0; rr_ < 4; ++rr_) {
        if (rr_ < 3) ldc((rr_ + 1) & 1, rr_ + 1);
#pragma unroll
        for (int d = 0; d < 8; ++d)
#pragma unroll
          for (int o = 0; o <= d; ++o) {
            if (d + o < 8) y[d + o] = mfma16(fa[rr_ & 1][d], fb[rr_ & 1][o], y[d + o]);
            if (o < d && d + o < 8) y[d + o] = mfma16(fa[rr_ & 1][o], fb[rr_ & 1][d], y[d + o]);
          }
        if (rr_ < 3) {
#pragma unroll
          for (int k = 0; k < 24; ++k) { SGB(0x8, 1); SGB(0x100, 1); }
        }
        sched0();
        if (rr_ == 2) { asm volatile("" ::: "memory"); prog_set(L, w, 20u); }
        sched0();
      }
    }
    LAS char* xo_ = L + L_KIMG + cl * 2 * CS + rh_ * 4096;
    LAS char* xi_ = L + L_KIMG + cl * 2 * CS + (rh_ ^ 1) * 4096;
    const unsigned pa_p = lbase + L_PROG + (w ^ 1) * 4;
    const unsigned yo = (unsigned)((((rh_ * 16 + (i16 >> 2)) * 8 + (b >> 5)) * 64 + ((2 * b + cl) & 63)) * 256 + g * 64 + (i16 & 3) * 16);
    auto finish = [&](auto keep) {
      constexpr int K = decltype(keep)::value;
      spin(pa_p, 20u);
#pragma unroll
      for (int t = 0; t < 4; ++t) *(LAS f32x4*)(xo_ + t * 1024 + lane * 16) = y[(K ^ 4) + t];
      asm volatile("" ::: "memory");
      prog_set(L, w, 21u);
      spin(pa_p, 21u);
      f32x4 yp[4];
#pragma unroll
      for (int t = 0; t < 4; ++t) yp[t] = *(LAS f32x4*)(xi_ + t * 1024 + lane * 16);
#pragma unroll
      for (int t = 0; t < 4; ++t) {
        const f32x4 o = yp[t];
        const v4u v = {pk2s(y[K + t][0] + o[0], y[K + t][1] + o[1]), tag, pk2s(y[K + t][2] + o[2], y[K + t][3] + o[3]), tag};
        st16c(ws + WS_YT + t * (4 * 8 * 64 * 256), yo, v);
      }
    };
    if (rh_) finish(IC<4>{}); else finish(IC<0>{});
    __builtin_amdgcn_s_setprio(0);
    spin_slow(lbase + L_PROG + (4 + w) * 4, 14u);
    v4u a0, a1, a2, a3;
    {
      const char* yb = ws + WS_YT + (m2 * 8 + n2) * 16384 + hw * 4096;
#pragma unroll 1
      for (unsigned sp = 0; sp < SPIN_CAP; ++sp) {
        ld16c(a0, yb, lane * 16); ld16c(a1, yb + 1024, lane * 16); ld16c(a2, yb + 2048, lane * 16); ld16c(a3, yb + 3072, lane * 16);
        waitv<0>(); PIN(a0); PIN(a1); PIN(a2); PIN(a3);
        if (__all(a0.y == tag && a0.w == tag && a1.y == tag && a1.w == tag && a2.y == tag && a2.w == tag && a3.y == tag && a3.w == tag)) break;
        __builtin_amdgcn_s_sleep(6);
      }
    }
    {
      LAS char* p = xi_ + g * 128 + ((i16 & 3) * 16 + (i16 >> 2) * 4) * 2;
      *(LAS v2u*)p = v2u{a0.x, a0.z}; *(LAS v2u*)(p + 512) = v2u{a1.x, a1.z}; *(LAS v2u*)(p + 1024) = v2u{a2.x, a2.z}; *(LAS v2u*)(p + 1536) = v2u{a3.x, a3.z};
    }
    {
      v4u av[2], xv[2];
#pragma unroll
      for (int it = 0; it < 2; ++it) {
        const int c8 = hw * 2 + it;
        LAS char* p = xi_ + (it * 8 + (i16 >> 2)) * 128 + (g * 16 + (i16 & 3) * 4) * 2;
        av[it] = trd2(p, p + 512);
        xv[it] = *(LAS v4u*)(L + L_BASE + lane * 128 + ((c8 ^ (lane & 7)) * 16));
      }
#pragma unroll
      for (int it = 0; it < 2; ++it) {
        v4u o;
#pragma unroll
        for (int e = 0; e < 4; ++e) o[e] = pk2s(bflo(xv[it][e]) + bflo(av[it][e]), bfhi(xv[it][e]) + bfhi(av[it][e]));
        *(LAS v4u*)(L + L_BASE + lane * 128 + (((hw * 2 + it) ^ (lane & 7)) * 16)) = o;
      }
    }
    asm volatile(".Lfk_e2:");
  } else {
    const unsigned pa_w = paddr(lbase, lane, 6, 1);
    const int kq = hw, r = lane & 31, h = lane >> 5;
    LAS char* W = L + kq * 2048;
    const int fb = min(22 * kq, 64);
    const int wa0 = (4 * h + (i16 >> 2)) * 128 + (i16 >> 3) * 64 + (g & 1) * 32 + (lane & 3) * 8;
    const int wa1 = wa0 ^ 64;
    const char* wds = (const char*)a.wd + (size_t)fb * 16384 + n2 * 128;
    int wi = rot2 + 5;
    wi = wi >= 22 ? wi - 22 : wi;
    auto wd_steps = [&](int n, int pos) {
#pragma unroll 1
      for (int i = 0; i < n; ++i) {
        dma2(wds + (size_t)wi * 16384, vd0, vd0 + 7168, lbase + pos + kq * 2048);
        wi = wi == 21 ? 0 : wi + 1;
        pos += 8192;
        waitv<WWIN>();
      }
    };
    if (w >= 6) {
      const int wk = w - 6, wt = tid - 384;
      v4u rr[8];
      unsigned epoch_w, dsk_v;
      bar();
      {
        const char* own = (const char*)a.x + (size_t)(t0 + 4 * wk) * 1024 + (jl & 1) * 512;
        const char* oth = (const char*)a.x + (size_t)(t0 + 4 * wk) * 1024 + 512 - (jl & 1) * 512;
        const unsigned vo = g * 1024 + i16 * 16, vo2 = vo + 256, vo3 = vo + 8192, vo4 = vo + 8448;
        ld16(rr[0], own, vo); ld16(rr[1], own, vo2); ld16(rr[2], oth, vo); ld16(rr[3], oth, vo2);
        ld16(rr[4], own, vo3); ld16(rr[5], own, vo4); ld16(rr[6], oth, vo3); ld16(rr[7], oth, vo4);
        ld4c(epoch_w, ws + WS_EPOCH, 0u);
        ld2(dsk_v, a.dsk, (unsigned)(2 * b + wk) * 2);
      }
      waitv<0>();
#pragma unroll
      for (int e = 0; e < 8; ++e) PIN(rr[e]);
      PIN(epoch_w); PIN(dsk_v);
      const unsigned epoch = __builtin_amdgcn_readfirstlane(epoch_w);
      const unsigned tag = epoch * 0x9E3779B1u + 0x7F4A7C15u;
      prog_set(L, 8, tag);
      v4u on[4];
      {
        float inv[2], nb[2];
#pragma unroll
        for (int q = 0; q < 2; ++q) {
          float s0 = 0.f, s1 = 0.f, q0 = 0.f, q1 = 0.f;
#pragma unroll
          for (int i = 0; i < 4; ++i) {
            s0 = dot2(rr[4 * q][i], 0x3F803F80u, s0); q0 = dot2(rr[4 * q][i], rr[4 * q][i], q0);
            s1 = dot2(rr[4 * q + 1][i], 0x3F803F80u, s1); q1 = dot2(rr[4 * q + 1][i], rr[4 * q + 1][i], q1);
            s0 = dot2(rr[4 * q + 2][i], 0x3F803F80u, s0); q0 = dot2(rr[4 * q + 2][i], rr[4 * q + 2][i], q0);
            s1 = dot2(rr[4 * q + 3][i], 0x3F803F80u, s1); q1 = dot2(rr[4 * q + 3][i], rr[4 * q + 3][i], q1);
          }
          const float mean = rsum16(cut(s0 + s1)) * (1.f / DM);
          const float var = fmaxf(rsum16(cut(q0 + q1)) * (1.f / DM) - mean * mean, 0.f);
          const float mu = rbf(mean), sd = rbf(__builtin_amdgcn_sqrtf(rbf(rbf(var) + 1e-5f)));
          inv[q] = __builtin_amdgcn_rcpf(sd); nb[q] = -mu * inv[q];
          if (i16 == 0 && !(jl & 1)) st8c(ws + WS_STAT, (unsigned)(t0 + 4 * wk + g + 8 * q) * 8, v2u{pk2(mu, sd), tag});
        }
#pragma unroll
        for (int k = 0; k < 2; ++k) {
          unsigned o[2][4];
#pragma unroll
          for (int q = 0; q < 2; ++q)
#pragma unroll
            for (int i = 0; i < 4; ++i) o[q][i] = pk2s(bflo(rr[4 * q + k][i]) * inv[q] + nb[q], bfhi(rr[4 * q + k][i]) * inv[q] + nb[q]);
          on[2 * k] = v4u{o[0][0], o[1][0], o[0][1], o[1][1]};
          on[2 * k + 1] = v4u{o[0][2], o[1][2], o[0][3], o[1][3]};
          PIN(on[2 * k]); PIN(on[2 * k + 1]);
        }
      }
      spin_slow(pa_c, 1u);
      f32x4 pt[2][4];
      {
        LAS char* p = L + L_UIMG + (4 * wk + g) * LNS + i16 * 32;
        *(LAS v4u*)p = on[0]; *(LAS v4u*)(p + 16) = on[1]; *(LAS v4u*)(p + 512) = on[2]; *(LAS v4u*)(p + 528) = on[3];
#pragma unroll
        for (int g4 = 0; g4 < 4; ++g4) { pt[0][g4] = *(LAS f32x4*)(L + part_off(2 * wk) + g4 * 1024 + lane * 16); pt[1][g4] = *(LAS f32x4*)(L + part_off(2 * wk + 1) + g4 * 1024 + lane * 16); }
      }
      asm volatile("" ::: "memory");
      prog_set(L, w, 1u);
      spin(pa_w, 1u);
      {
        LAS char* xp = L + L_UIMG + (i16 >> 2) * LNS + (wk * 64 + g * 4 + (lane & 3)) * 8;
        const unsigned xto = (unsigned)(((jl & 1) * 128 + wk * 64 + g * 4 + (i16 >> 2)) * 8192 + (t0 >> 4) * 64 + (lane & 3) * 16);
        v4u xv[4];
#pragma unroll
        for (int it = 0; it < 4; ++it) xv[it] = trd2(xp + it * 128, xp + it * 128 + 4 * LNS);
#pragma unroll
        for (int it = 0; it < 4; ++it) st16c(ws + WS_XT + it * 16 * 8192, xto, xv[it]);
      }
      {
        float v[16];
#pragma unroll
        for (int e = 0; e < 16; ++e) v[e] = cut(pt[0][e >> 2][e & 3] + pt[1][e >> 2][e & 3]);
        if (lane == 0) v[0] += bf2f(dsk_v);
        const int B = lane & 31, hh = lane >> 5;
        LAS char* k0 = L + L_KIMG + wk * 2 * CS + (255 - B) * 16;
        LAS char* ka = k0 - hh * 512;
        *(LAS v4u*)ka = v4u{pk2(v[7], v[6]), pk2(v[5], v[4]), pk2(v[3], v[2]), pk2(v[1], v[0])};
        *(LAS v4u*)(ka + CS) = v4u{pk2(v[8], v[7]), pk2(v[6], v[5]), pk2(v[4], v[3]), pk2(v[2], v[1])};
        const unsigned p00 = pk2(v[9], v[9]), p44 = pk2(v[10], v[10]), p88 = pk2(v[11], v[11]);
        *(LAS v4u*)(ka - 1024) = v4u{p88, p44, pk2(v[10], v[9]), p00};
        *(LAS v4u*)(ka - 1024 + CS) = v4u{p88, pk2(v[11], v[10]), p44, p00};
        LAS char* kc = k0 - 2048 - hh * 1024;
#pragma unroll
        for (int t = 0; t < 2; ++t) {
          const float lo = v[12 + 2 * t], hi = v[13 + 2 * t], md = (B & 1) ? hi : lo;
          const unsigned ll = pk2(lo, lo), hh2 = pk2(hi, hi);
          *(LAS v4u*)(kc - t * 512) = v4u{hh2, pk2(hi, md), ll, ll};
          *(LAS v4u*)(kc - t * 512 + CS) = v4u{hh2, hh2, pk2(md, lo), ll};
        }
        if (lane < 4) {
          *(LAS v4u*)(L + L_KIMG + wk * 2 * CS + 4096 + lane * 16) = v4u{0u, 0u, 0u, 0u};
          *(LAS v4u*)(L + L_KIMG + wk * 2 * CS + CS + 4096 + lane * 16) = v4u{lane == 0 ? pk2(v[0], 0.f) : 0u, 0u, 0u, 0u};
        }
      }
      waitv<0>();
      prog_done(L, w, 2u);
      if (__all(peek(pa_w) >= 2u)) { if (lane == 0) st4c(ws + WS_F1, (unsigned)b * 4, tag); }
      if (j == 15) { for (int i = 0; i < 6; ++i) __builtin_amdgcn_s_sleep(16); }
      else spin_slow(lbase + L_PROG + 16, 7u);
      v4u pfl, pst;
      ld16c(pst, ws + WS_STAT + m * 1024, lane * 16); ld16c(pfl, ws + WS_F1, lane * 16);
      waitv<0>(); PIN(pst); PIN(pfl);
      if (wk == 0) {
#pragma unroll 1
        for (unsigned sp = 0; sp < SPIN_CAP; ++sp) {
          if (__all(pst.y == tag && pst.w == tag)) break;
          __builtin_amdgcn_s_sleep(8);
          ld16c(pst, ws + WS_STAT + m * 1024, lane * 16); waitv<0>(); PIN(pst);
        }
        *(LAS v2u*)(L + L_STAT + lane * 8) = v2u{pst.x, pst.z};
        asm volatile("" ::: "memory");
        prog_set(L, w, 3u);
        asm volatile(".Lfk_e3:");
      }
#pragma unroll 1
      for (unsigned sp = 0; sp < SPIN_CAP; ++sp) {
        if (__all(pfl.x == tag && pfl.y == tag && pfl.z == tag && pfl.w == tag)) break;
        __builtin_amdgcn_s_sleep(8);
        ld16c(pfl, ws + WS_F1, lane * 16); waitv<0>(); PIN(pfl);
      }
      if (j < 15) spin(lbase + L_PROG + P_MARK, 2u);
      v4u u0, u1, u2, u3;
      const char* xb = ws + WS_XT + b * 8192;
      ld16c(u0, xb, wt * 16); ld16c(u1, xb + 2048, wt * 16); ld16c(u2, xb + 4096, wt * 16); ld16c(u3, xb + 6144, wt * 16);
      {
        const unsigned vo = (unsigned)((lane >> 3) * 1024 + (lane & 7) * 16);
        const char* xt = (const char*)a.x + (size_t)(m2 * 64 + wk * 32) * 1024 + n2 * 128;
        ld16(rr[0], xt, vo); ld16(rr[1], xt + 8192, vo); ld16(rr[2], xt + 16384, vo); ld16(rr[3], xt + 24576, vo);
      }
      if (b == 0 && wt == 0) st4c(ws + WS_EPOCH, 0u, epoch + 1u);
      LAS char* up = L + L_UIMG + (wt & 1) * US + 512 + ((wt >> 2) * 16 + ((wt >> 1) & 1) * 8) * 2;
      if (lane < 32) *(LAS v4u*)(L + L_UIMG + wk * US + lane * 16) = v4u{0u, 0u, 0u, 0u};
      waitv<0>(); PIN(u0); PIN(u1); PIN(u2); PIN(u3);
      *(LAS v4u*)up = u0; *(LAS v4u*)(up + 1024) = u1; *(LAS v4u*)(up + 2048) = u2; *(LAS v4u*)(up + 3072) = u3;
#pragma unroll
      for (int e = 0; e < 4; ++e) { PIN(rr[e]); *(LAS v4u*)(L + L_BASE + wk * 4096 + e * 1024 + (lane >> 3) * 128 + (((lane & 7) ^ (lane >> 3)) * 16)) = rr[e]; }
      asm volatile("" ::: "memory");
      prog_set(L, w, 5u);
      asm volatile(".Lfk_e1:");
    } else {
      __builtin_amdgcn_s_setprio(1);
      unsigned vx[8], vw[12];
      {
        const int r = lane >> 3, cp = lane & 7;
#pragma unroll
        for (int i = 0; i < 12; ++i) { const int n = (i % 3) * 64 + lane; vw[i] = ((i / 3) * 16 + n / 12) * (FF * 2) + (n % 12) * 16 - (i & 3) * 1024; PIN(vw[i]); }
#pragma unroll
        for (int i = 0; i < 8; ++i) { vx[i] = r * 1024 + ((cp ^ ((i & 1) * 4 + g)) * 16) + i * 8192 - (i & 3) * 1024; PIN(vx[i]); }
      }
      bar();
      const int rot1 = (m + 2 * j) & 7;
      const char* xs0 = (const char*)a.x + (size_t)(m * 128) * 1024;
      const char* gs0 = (const char*)a.wg + f0 * 2;
      const char* us0 = (const char*)a.wu + f0 * 2;
      auto stage = [&](auto pace, int s, int sl) {
        constexpr int P = decltype(pace)::value;
        const unsigned lb = lbase + sl * RS;
        const int ks = (s + rot1) & 7;
        dma12<P>(gs0 + (size_t)ks * (64 * FF * 2), vw, lb + WGO);
        dma8<P>(xs0 + ks * 128, vx, lb);
        dma12<P>(us0 + (size_t)ks * (64 * FF * 2), vw, lb + WUO);
        dma8<P>(xs0 + ks * 128 + 65536, vx, lb + 8192);
      };
      if (j < 15) {
        if (hw) spin(lbase + L_PROG + P_MARK, 1u);
        stage(IC<0>{}, hw, hw);
        if (!hw) put1(lbase + L_PROG + P_MARK, 1u);
        waitv<0>();
        prog_set(L, w, hw + 1);
        int sl = hw ? 0 : 2;
#pragma unroll 1
        for (int it = 1; it < 4; ++it) {
          const int s = 2 * it + hw;
          if (s >= 3) spin(pa_c, s - 1);
          stage(IC<PACE>{}, s, sl);
          sl = sl == 0 ? 2 : sl - 1;
          if (s == 7) put1(lbase + L_PROG + P_MARK, 2u);
          waitv<0>();
          prog_set(L, w, s + 1);
        }
      }
      if (!hw) {
        unsigned vd[8];
#pragma unroll
        for (int i = 0; i < 8; ++i) vd[i] = vd0 + (unsigned)(min(22 * (i >> 1), 64) * 16384 + (i & 1) * 8192 - (i & 3) * 1024);
        if (j < 15) { spin(lbase + L_PROG + P_MARK, 2u); __builtin_amdgcn_s_sleep(3); }
        spin(pa_c, 7u);
        int ii = rot2;
        {
          const int i6 = rot2 + 5 >= 22 ? rot2 - 17 : rot2 + 5;
          touch((const char*)a.wd + (size_t)i6 * 16384 + n2 * 128, (unsigned)(min(22 * g, 64) * 16 + i16) * 1024, lbase + L_CSUM + 3072);
        }
#pragma unroll 1
        for (int i = 0; i < 5; ++i) {
          ii = ii >= 22 ? ii - 22 : ii;
          dma8<0>((const char*)a.wd + (size_t)ii * 16384 + n2 * 128, vd, lbase + 2 * RS + i * 8192);
          ++ii;
        }
      }
      __builtin_amdgcn_s_setprio(0);
    }
    LAS char* wp[3][2];
#pragma unroll
    for (int k = 0; k < 3; ++k) { wp[k][0] = W + wd_pos(5 * k) + wa0; wp[k][1] = W + wd_pos(5 * k) + wa1; PIN(wp[k][0]); PIN(wp[k][1]); }
    auto wfrag = [&](v4u* d, int i) {
      d[0] = trd2(wp[i / 5][0] + (i % 5) * 8192, wp[i / 5][0] + (i % 5) * 8192 + 1024); d[1] = trd2(wp[i / 5][1] + (i % 5) * 8192, wp[i / 5][1] + (i % 5) * 8192 + 1024);
    };
    LAS char* pw_[8];
#pragma unroll
    for (int k = 0; k < 8; ++k) { pw_[k] = W + (r >> 3) * 8192 + (r & 7) * 256 + (((2 * k + h) ^ (r & 15)) * 16); PIN(pw_[k]); }
    spin_slow(lbase + L_PROG + 24, 1u);
    unsigned tag = __builtin_amdgcn_readfirstlane(peek(lbase + L_PROG + P_TAG));
    unsigned fo = (unsigned)min((22 * kq) / 3 + lane, (22 * kq + 21) / 3) * 128;
    const char* fbase = ws + WS_F2 + m2 * 4096;
    PIN(fo); asm volatile("" : "+s"(tag), "+s"(fbase));
    __builtin_amdgcn_s_setprio(2);
    if (w == 4) { waitv<0>(); prog_set(L, w, 9u); } else spin_slow(lbase + L_PROG + 16, 9u);
    v4u we[7][2];
#pragma unroll
    for (int i = 0; i < 5; ++i) wfrag(we[i], i);
    spin(pa_c, 10u);
    wd_steps(5, 0);
    wd_steps(5, RS);
#pragma unroll
    for (int i = 5; i < 7; ++i) wfrag(we[i], i);
    wd_steps(4, 2 * RS);
    unsigned fv;
    ld4c(fv, fbase, fo);
    wd_steps(1, 2 * RS + 4 * 8192);
#pragma unroll
    for (int i = 0; i < 7; ++i) { PIN(we[i][0]); PIN(we[i][1]); }
    waitl();
    wd_steps(2, 0);
    waitv<4>(); PIN(fv);
    const bool ready = __all(fv == tag);
    const __amdgpu_buffer_rsrc_t rh = __builtin_amdgcn_make_buffer_rsrc((void*)(ws + WS_H), 0, H_BYTES, 0x00020000);
    if (j == 15) spin_slow(pa_c, 20u);
    if (!ready) poll_eq(fbase, fo, tag);
    __builtin_amdgcn_s_setprio(0);
    v4u hf[HW][2];
    auto ldh = [&](int i) {
      int ii = i + rot2; ii = ii >= 22 ? ii - 22 : ii;
      const int F16 = fb + ii;
      const int so = ((m2 * 2 * 86 + F16) * 1024) | (((F16 - 22 * kq) >> 31) & 0x40000000);
      hf[i % HW][0] = __builtin_amdgcn_raw_buffer_load_b128(rh, lane * 16, so, 16);
      hf[i % HW][1] = __builtin_amdgcn_raw_buffer_load_b128(rh, lane * 16, so + 86 * 1024, 16);
    };
#pragma unroll
    for (int i = 0; i < HW0; ++i) ldh(i);
    sched0();
    f32x16 o2[2][2];
#pragma unroll
    for (int i = 0; i < 4; ++i) o2[i >> 1][i & 1] = f32x16{};
    v4u wn[2][2];
    constexpr int NT = 22 - TAILN;
    int nld = HW0;
#pragma unroll
    for (int i = 0; i < NT; ++i) {
      if (i + 1 >= 7 && i + 1 < NT) {
        wfrag(wn[(i + 1) & 1], i + 1 < 15 ? i + 1 : i + 1 - 15);
      }
      if (i >= NT - TAILN) {
        const int k = i - (NT - TAILN);
        wfrag(we[k], NT + k - 15);
      }
      const v4u w0 = i < 7 ? we[i < 7 ? i : 0][0] : wn[i & 1][0], w1 = i < 7 ? we[i < 7 ? i : 0][1] : wn[i & 1][1];
      const v4u h0 = hf[i % HW][0], h1 = hf[i % HW][1];
      o2[0][0] = mfma32(w0, h0, o2[0][0]); o2[1][0] = mfma32(w1, h0, o2[1][0]);
      o2[0][1] = mfma32(w0, h1, o2[0][1]); o2[1][1] = mfma32(w1, h1, o2[1][1]);
#pragma unroll
      for (int k = 0; k < 2; ++k) if (nld < 22 && nld < i + HW) { ldh(nld); ++nld; }
      if (i == YAT) prog_set(L, w, 14u);
      SGB(0x8, 1); SGB(0x100, 2); SGB(0x20, 1); SGB(0x8, 1); SGB(0x100, 2); SGB(0x20, 1); SGB(0x8, 1); SGB(0x100, 2); SGB(0x20, 1); SGB(0x8, 1); SGB(0x100, 2); SGB(0x20, 1);
      sched0();
    }
    asm volatile("" ::: "memory");
    auto wr = [&](int cc, int tt) {
#pragma unroll
      for (int g4 = 0; g4 < 4; ++g4)
        *(LAS f32x4*)(pw_[cc * 4 + g4] + tt * RS) = f32x4{o2[cc][tt][4 * g4], o2[cc][tt][4 * g4 + 1], o2[cc][tt][4 * g4 + 2], o2[cc][tt][4 * g4 + 3]};
    };
#pragma unroll
    for (int q = 0; q < 4; ++q) {
      const int cc = q & 1, tt = q >> 1;
#pragma unroll
      for (int k = 0; k < TAILN; ++k) o2[cc][tt] = mfma32(we[k][cc], hf[(NT + k) % HW][tt], o2[cc][tt]);
      if (q > 0) { wr((q - 1) & 1, (q - 1) >> 1); SGB(0x8, 1); SGB(0x200, 1); SGB(0x8, 1); SGB(0x200, 1); SGB(0x8, 1); SGB(0x200, 1); SGB(0x8, 1); SGB(0x200, 1); SGB(0x8, 2); }
      sched0();
    }
    wr(1, 1);
  }
  {
    const int tok = tid >> 3, c8 = tid & 7;
    LAS char* P = L + (tok >> 5) * RS + ((tok >> 3) & 3) * 8192 + (tok & 7) * 256;
    LAS char* p0 = P + (((2 * c8) ^ (tok & 15)) * 16);
    LAS char* p1 = P + (((2 * c8 + 1) ^ (tok & 15)) * 16);
    LAS char* pb = L + L_BASE + tok * 128 + ((c8 ^ (tok & 7)) * 16);
    unsigned oo = tok * 1024 + c8 * 16;
    char* ob = (char*)a.out + (size_t)(m2 * 64) * 1024 + n2 * 128;
    PIN(p0); PIN(p1); PIN(pb); PIN(oo); asm volatile("" : "+s"(ob));
    bar();
    asm volatile("" :: "s"(ka_touch));
    f32x4 q0[4], q1[4];
#pragma unroll
    for (int k = 0; k < 4; ++k) { q0[k] = *(LAS f32x4*)(p0 + k * 2048); q1[k] = *(LAS f32x4*)(p1 + k * 2048); }
    const v4u bv = *(LAS v4u*)pb;
    sched0();
    const f32x4 s0 = (q0[0] + q0[1]) + (q0[2] + q0[3]), s1 = (q1[0] + q1[1]) + (q1[2] + q1[3]);
    const unsigned f0_ = pk2(s0[0], s0[1]), f1_ = pk2(s0[2], s0[3]), f2_ = pk2(s1[0], s1[1]), f3_ = pk2(s1[2], s1[3]);
    const v4u o = {pk2(bflo(bv.x) + bflo(f0_), bfhi(bv.x) + bfhi(f0_)), pk2(bflo(bv.y) + bflo(f1_), bfhi(bv.y) + bfhi(f1_)),
                   pk2(bflo(bv.z) + bflo(f2_), bfhi(bv.z) + bfhi(f2_)), pk2(bflo(bv.w) + bflo(f3_), bfhi(bv.w) + bfhi(f3_))};
    asm volatile("global_store_dwordx4 %0, %1, %2 sc0 sc1\n\ts_nop 1" :: "v"(oo), "v"(o), "s"(ob) : "memory");
  }
  asm volatile(".Lfk_end:");
}

extern "C" void kernel_launch(void* const* d_in, const int* in_sizes, int n_in, void* d_out, int out_size, void* d_ws, size_t ws_size,
                              hipStream_t stream) {
  Args a;
  a.x = (const u16*)d_in[0]; a.are = (const u16*)d_in[1]; a.aim = (const u16*)d_in[2]; a.bre = (const u16*)d_in[3]; a.bim = (const u16*)d_in[4];
  a.cre = (const u16*)d_in[5]; a.cim = (const u16*)d_in[6]; a.logdt = (const u16*)d_in[7]; a.dsk = (const u16*)d_in[8];
  a.wg = (const u16*)d_in[10]; a.wu = (const u16*)d_in[11]; a.wd = (const u16*)d_in[12];
  a.out = (u16*)d_out; a.ws = (char*)d_ws;
  (void)hipFuncSetAttribute((const void*)fused_kernel, hipFuncAttributeMaxDynamicSharedMemorySize, L_TOTAL);
  fused_kernel<<<256, 512, L_TOTAL, stream>>>(a);
}
```

```cpp
#include <hip/hip_runtime.h>
#include <hip/hip_bf16.h>

typedef unsigned short u16;
typedef unsigned v4u __attribute__((ext_vector_type(4)));
typedef unsigned v2u __attribute__((ext_vector_type(2)));
typedef short s16x4 __attribute__((ext_vector_type(4)));
typedef __bf16 bf16x8 __attribute__((ext_vector_type(8)));
typedef __bf16 bf16x2 __attribute__((ext_vector_type(2)));
typedef float f32x2 __attribute__((ext_vector_type(2)));
typedef float f32x4 __attribute__((ext_vector_type(4)));
typedef float f32x16 __attribute__((ext_vector_type(16)));
#define LAS __attribute__((address_space(3)))
#define DEVI __device__ __forceinline__

constexpr int DM = 512, FF = 1376;
constexpr int WS_EPOCH = 0;
constexpr int WS_F1 = 4096;
constexpr int WS_F2 = 1 << 18;
constexpr int WS_STAT = 16384;
constexpr int WS_XT = 1 << 20;
constexpr int WS_YT = 3 << 20;
constexpr int WS_H = 7 << 20;
constexpr int H_BYTES = 64 * 86 * 1024;
constexpr int RS = 41024, WGO = 16384, WUO = 16384 + 12320;
constexpr int CS = 4160;
constexpr int L_KIMG = 3 * RS;
constexpr int L_UIMG = L_KIMG + 4 * CS;
constexpr int US = 4608;
constexpr int L_ZT = L_KIMG, ZTS = 10064;
constexpr int L_CSUM = L_KIMG + 36864;
constexpr int L_PROG = L_ZT + 4 * ZTS;
constexpr int L_TOTAL = L_PROG + 64;
static_assert(L_TOTAL <= 163840, "lds");
DEVI int part_off(int w) { return L_ZT + (w == 2 ? 25856 : w * ZTS); }
constexpr int LNS = 1088;
constexpr int L_STAT = L_KIMG + 34304;
constexpr int L_BASE = L_KIMG + 25856;
constexpr unsigned SPIN_CAP = 1u << 17;
constexpr int PACE = 3, CAP = 12;
constexpr int WWIN = 4;
constexpr int HW = 8, HW0 = 4, TAILN = 6;
constexpr int P_TAG = 32, P_MARK = 36, YAT = 12;
constexpr int wd_pos(int i) { return ((i / 5 + 2) % 3) * RS + (i % 5) * 8192; }

DEVI float bf2f(unsigned b) { return __builtin_bit_cast(float, b << 16); }
DEVI float bflo(unsigned w) { return __builtin_bit_cast(float, w << 16); }
DEVI float bfhi(unsigned w) { return __builtin_bit_cast(float, w & 0xffff0000u); }
DEVI unsigned pk2v(f32x2 v) { return __builtin_bit_cast(unsigned, __builtin_convertvector(v, bf16x2)); }
DEVI unsigned pk2(float a, float b) { return pk2v(f32x2{a, b}); }
DEVI float rbf(float a) { return bflo(pk2(a, a)); }
DEVI f32x16 mfma32(v4u a, v4u b, f32x16 c) { return __builtin_amdgcn_mfma_f32_32x32x16_bf16(__builtin_bit_cast(bf16x8, a), __builtin_bit_cast(bf16x8, b), c, 0, 0, 0); }
DEVI f32x4 mfma16(v4u a, v4u b, f32x4 c) { return __builtin_amdgcn_mfma_f32_16x16x32_bf16(__builtin_bit_cast(bf16x8, a), __builtin_bit_cast(bf16x8, b), c, 0, 0, 0); }
DEVI v2u trd(LAS char* p) { return __builtin_bit_cast(v2u, __builtin_amdgcn_ds_read_tr16_b64_v4i16((LAS s16x4*)p)); }
DEVI v4u trd2(LAS char* p0, LAS char* p1) { v2u a = trd(p0), b = trd(p1); return v4u{a.x, a.y, b.x, b.y}; }
template <int N> DEVI void waitv() { asm volatile("s_waitcnt vmcnt(%0)" :: "i"(N) : "memory"); }
DEVI void waitl() { asm volatile("" ::: "memory"); __builtin_amdgcn_s_waitcnt(0xC07F); asm volatile("" ::: "memory"); }
DEVI void bar() { waitl(); asm volatile("s_waitcnt lgkmcnt(0)\n\ts_barrier" ::: "memory"); }
DEVI void sched0() { __builtin_amdgcn_sched_barrier(0); }
#define SGB(mask, n) __builtin_amdgcn_sched_group_barrier(mask, n, 0)
DEVI void ld16(v4u& r, const void* sbase, unsigned voff) { asm volatile("global_load_dwordx4 %0, %1, %2" : "=v"(r) : "v"(voff), "s"(sbase) : "memory"); }
DEVI void ld16c(v4u& r, const void* sbase, unsigned voff) { asm volatile("global_load_dwordx4 %0, %1, %2 sc1" : "=v"(r) : "v"(voff), "s"(sbase) : "memory"); }
DEVI void ld4c(unsigned& r, const void* sbase, unsigned voff) { asm volatile("global_load_dword %0, %1, %2 sc1" : "=v"(r) : "v"(voff), "s"(sbase) : "memory"); }
DEVI void ld2(unsigned& r, const void* sbase, unsigned voff) { asm volatile("global_load_ushort %0, %1, %2" : "=v"(r) : "v"(voff), "s"(sbase) : "memory"); }
DEVI void st16c(void* sbase, unsigned voff, v4u v) { asm volatile("global_store_dwordx4 %0, %1, %2 sc1\n\ts_nop 1" :: "v"(voff), "v"(v), "s"(sbase) : "memory"); }
DEVI void st8c(void* sbase, unsigned voff, v2u v) { asm volatile("global_store_dwordx2 %0, %1, %2 sc1\n\ts_nop 1" :: "v"(voff), "v"(v), "s"(sbase) : "memory"); }
DEVI void st4c(void* sbase, unsigned voff, unsigned v) { asm volatile("global_store_dword %0, %1, %2 sc1\n\ts_nop 1" :: "v"(voff), "v"(v), "s"(sbase) : "memory"); }
DEVI void flag1(void* sbase, unsigned voff, unsigned v) {
  asm volatile("s_mov_b64 exec, 1\n\tglobal_store_dword %0, %1, %2 sc1\n\ts_mov_b64 exec, -1" :: "v"(voff), "v"(v), "s"(sbase) : "memory");
}
#define PIN(x) asm volatile("" : "+v"(x))
template <int C> DEVI float dppf(float v) { return __builtin_bit_cast(float, __builtin_amdgcn_update_dpp(0, __builtin_bit_cast(int, v), C, 0xf, 0xf, true)); }
DEVI float rsum16(float v) { v += dppf<0xB1>(v); v += dppf<0x4E>(v); v += dppf<0x141>(v); v += dppf<0x140>(v); return v; }
DEVI float cut(float x) { asm("" : "+v"(x)); return x; }
DEVI unsigned pk2s(float a, float b) { return pk2(cut(a), cut(b)); }
DEVI float dot2(unsigned a, unsigned b, float c) { return __builtin_amdgcn_fdot2_f32_bf16(__builtin_bit_cast(bf16x2, a), __builtin_bit_cast(bf16x2, b), c, false); }
DEVI void dma2(const void* sbase, unsigned v0, unsigned v1, unsigned ldsaddr) {
  asm volatile("s_mov_b32 m0, %3\n\ts_nop 0\n\tglobal_load_lds_dwordx4 %0, %2\n\tglobal_load_lds_dwordx4 %1, %2 offset:1024" :: "v"(v0), "v"(v1), "s"(sbase), "s"(ldsaddr) : "memory");
}
#define DMA4(a, b, c, d, sb, lb, off, p, q) \
  "s_waitcnt vmcnt(%" #q ")\n\ts_add_i32 m0, %" #lb ", " #off "\n\ts_nop %" #p "\n\tglobal_load_lds_dwordx4 %" #a ", %" #sb "\n\ts_nop %" #p "\n\tglobal_load_lds_dwordx4 %" #b ", %" #sb " offset:1024\n\t" \
  "s_nop %" #p "\n\tglobal_load_lds_dwordx4 %" #c ", %" #sb " offset:2048\n\ts_nop %" #p "\n\tglobal_load_lds_dwordx4 %" #d ", %" #sb " offset:3072\n\t"
template <int P> DEVI void dma8(const void* sbase, const unsigned* v, unsigned lb) {
  asm volatile(DMA4(0, 1, 2, 3, 8, 9, 0, 10, 11) DMA4(4, 5, 6, 7, 8, 9, 4096, 10, 11)
               :: "v"(v[0]), "v"(v[1]), "v"(v[2]), "v"(v[3]), "v"(v[4]), "v"(v[5]), "v"(v[6]), "v"(v[7]), "s"(sbase), "s"(lb), "i"(P), "i"(P ? CAP : 63) : "memory", "scc");
}
template <int P> DEVI void dma12(const void* sbase, const unsigned* v, unsigned lb) {
  asm volatile(DMA4(0, 1, 2, 3, 12, 13, 0, 14, 15) DMA4(4, 5, 6, 7, 12, 13, 4096, 14, 15) DMA4(8, 9, 10, 11, 12, 13, 8192, 14, 15)
               :: "v"(v[0]), "v"(v[1]), "v"(v[2]), "v"(v[3]), "v"(v[4]), "v"(v[5]), "v"(v[6]), "v"(v[7]), "v"(v[8]), "v"(v[9]), "v"(v[10]), "v"(v[11]),
                  "s"(sbase), "s"(lb), "i"(P), "i"(P ? CAP : 63) : "memory", "scc");
}
DEVI void touch(const void* sbase, unsigned voff, unsigned ldsdump) {
  asm volatile("s_mov_b32 m0, %2\n\ts_nop 0\n\tglobal_load_lds_dword %0, %1" :: "v"(voff), "s"(sbase), "s"(ldsdump) : "memory");
}
#define CMM(r, a, x) "v_pk_mul_f32 %" #r ", %" #a ", %" #x " op_sel:[1,1] op_sel_hi:[1,0]\n\t"
#define CMF(r, a, x) "v_pk_fma_f32 %" #r ", %" #a ", %" #x ", %" #r " op_sel_hi:[0,1,1] neg_lo:[0,0,1]\n\t"
DEVI void cm4(f32x2& r0, f32x2& r1, f32x2& r2, f32x2& r3, f32x2 a0, f32x2 x0, f32x2 a1, f32x2 x1, f32x2 a2, f32x2 x2, f32x2 a3, f32x2 x3) {
  asm(CMM(0, 4, 5) CMM(1, 6, 7) CMM(2, 8, 9) CMM(3, 10, 11) CMF(0, 4, 5) CMF(1, 6, 7) CMF(2, 8, 9) CMF(3, 10, 11)
      : "=&v"(r0), "=&v"(r1), "=&v"(r2), "=&v"(r3) : "v"(a0), "v"(x0), "v"(a1), "v"(x1), "v"(a2), "v"(x2), "v"(a3), "v"(x3));
}
DEVI void put1(unsigned addr, unsigned v) {
  asm volatile("s_mov_b64 exec, 1\n\tds_write_b32 %0, %1\n\ts_mov_b64 exec, -1" :: "v"(addr), "v"(v) : "memory");
}
DEVI void prog_set(LAS char* L, int w, unsigned v) { put1((unsigned)(size_t)L + L_PROG + w * 4, v); }
DEVI void prog_done(LAS char* L, int w, unsigned v) { waitl(); prog_set(L, w, v); }
DEVI unsigned paddr(unsigned lbase, int lane, int w0, int mask) { return lbase + L_PROG + (w0 + (lane & mask)) * 4; }
DEVI void spin(unsigned addr, unsigned n) {
  unsigned t, c = 1u << 22;
  asm volatile("1:\n\tds_read_b32 %0, %2\n\ts_sub_u32 %1, %1, 1\n\ts_waitcnt lgkmcnt(0)\n\tv_cmp_gt_u32 vcc, %3, %0\n\ts_cselect_b64 vcc, 0, vcc\n\ts_cbranch_vccnz 1b"
               : "=&v"(t), "+s"(c) : "v"(addr), "s"(n) : "memory", "vcc", "scc");
}
DEVI void spin_if(unsigned seen, unsigned addr, unsigned n) {
  unsigned t, c;
  asm volatile("s_cmp_ge_u32 %4, %3\n\ts_cbranch_scc1 2f\n\ts_mov_b32 %1, 0x400000\n"
               "1:\n\tds_read_b32 %0, %2\n\ts_sub_u32 %1, %1, 1\n\ts_waitcnt lgkmcnt(0)\n\tv_cmp_gt_u32 vcc, %3, %0\n\ts_cselect_b64 vcc, 0, vcc\n\ts_cbranch_vccnz 1b\n2:"
               : "=&v"(t), "=&s"(c) : "v"(addr), "s"(n), "s"(seen) : "memory", "vcc", "scc");
}
DEVI void spin_slow(unsigned addr, unsigned n) {
  unsigned t, c = 1u << 21;
  asm volatile("1:\n\tds_read_b32 %0, %2\n\ts_waitcnt lgkmcnt(0)\n\tv_cmp_gt_u32 vcc, %3, %0\n\ts_cbranch_vccz 2f\n\ts_sleep 2\n\ts_sub_u32 %1, %1, 1\n\ts_cbranch_scc0 1b\n2:"
               : "=&v"(t), "+s"(c) : "v"(addr), "s"(n) : "memory", "vcc", "scc");
}
DEVI void poll_eq(const void* sbase, unsigned voff, unsigned want) {
  unsigned t, c = SPIN_CAP;
  asm volatile("1:\n\tglobal_load_dword %0, %2, %3 sc1\n\ts_sub_u32 %1, %1, 1\n\ts_waitcnt vmcnt(0)\n\tv_cmp_ne_u32 vcc, %4, %0\n\ts_cselect_b64 vcc, 0, vcc\n\ts_cbranch_vccnz 1b"
               : "=&v"(t), "+s"(c) : "v"(voff), "s"(sbase), "s"(want) : "memory", "vcc", "scc");
}
DEVI unsigned peek(unsigned addr) { return *(volatile LAS unsigned*)(size_t)addr; }
template <int N> struct IC { static constexpr int value = N; };
struct Args {
  const u16 *x, *are, *aim, *bre, *bim, *cre, *cim, *logdt, *dsk, *wg, *wu, *wd;
  u16* out;
  char* ws;
};

__global__ void __launch_bounds__(512, 2) fused_kernel(Args a) {
  extern __shared__ __attribute__((aligned(16))) char smem[];
  LAS char* L = (LAS char*)smem;
  const unsigned lbase = (unsigned)(size_t)L;
  unsigned ka_touch;
  asm volatile("s_load_dword %0, %1, 0x0" : "=s"(ka_touch) : "s"(__builtin_amdgcn_kernarg_segment_ptr()));
  unsigned long long pc; unsigned sz;
  asm volatile("s_getpc_b64 %0\n.Lfk_start:\n\ts_mov_b32 %1, .Lfk_end-.Lfk_start\n\ts_max_u32 %1, %1, .Lfk_e1-.Lfk_start\n\ts_max_u32 %1, %1, .Lfk_e2-.Lfk_start\n\ts_max_u32 %1, %1, .Lfk_e3-.Lfk_start"
               : "=s"(pc), "=s"(sz) :: "scc");
  const int tid = threadIdx.x, lane = tid & 63;
  int w = __builtin_amdgcn_readfirstlane(tid >> 6);
  const int b = blockIdx.x, xcd = b & 7, slot = b >> 3;
  unsigned codetouch = 0;
  {
    const unsigned off = (unsigned)((lane * 8 + (slot & 7)) * 128);
    if (w == 0 && off < sz) asm volatile("global_load_dword %0, %1, %2" : "=v"(codetouch) : "v"(off), "s"(pc) : "memory");
  }
  prog_set(L, w, 0u); prog_set(L, w + 8, 0u);
  unsigned pq[7], epoch_v;
  if (w < 4) {
    const unsigned po = (unsigned)(b * 256 + tid) * 2;
    ld2(pq[6], a.logdt, (unsigned)(2 * b + (w >> 1)) * 2);
    ld2(pq[0], a.are, po); ld2(pq[1], a.aim, po); ld2(pq[2], a.bre, po); ld2(pq[3], a.bim, po); ld2(pq[4], a.cre, po); ld2(pq[5], a.cim, po);
    ld4c(epoch_v, a.ws + WS_EPOCH, 0u);
  }
  const int jl = (xcd & 3) * 4 + (slot & 3);
  const int t0 = ((xcd >> 2) * 8 + (slot >> 2)) * 128 + (jl >> 1) * 16;
  v4u rr[8];
  unsigned epoch_w, dsk_v;
#pragma unroll
  for (int e = 0; e < 8; ++e) asm volatile("" : "=v"(rr[e]));
  const char* own = (const char*)a.x + (size_t)(t0 + 4 * (w & 1)) * 1024 + (jl & 1) * 512;
  const char* oth = (const char*)a.x + (size_t)(t0 + 4 * (w & 1)) * 1024 + 512 - (jl & 1) * 512;
  unsigned vo = (lane >> 4) * 1024 + (lane & 15) * 16, vo2 = vo + 256, vo3 = vo + 8192, vo4 = vo + 8448;
  PIN(vo); PIN(vo2); PIN(vo3); PIN(vo4); asm volatile("" : "+s"(own), "+s"(oth));
  bar();
  if (w >= 6) {
    ld16(rr[0], own, vo); ld16(rr[1], own, vo2); ld16(rr[2], oth, vo); ld16(rr[3], oth, vo2);
    ld16(rr[4], own, vo3); ld16(rr[5], own, vo4); ld16(rr[6], oth, vo3); ld16(rr[7], oth, vo4);
    ld4c(epoch_w, a.ws + WS_EPOCH, 0u);
    ld2(dsk_v, a.dsk, (unsigned)(2 * b + (w & 1)) * 2);
  }
  asm volatile("" : "+s"(w));
  const int hw = w & 3;
  const int tl = 30 * (xcd & 3) + slot;
  const int m = (xcd >> 2) * 8 + (tl & 7), j = slot < 30 ? tl >> 3 : 15;
  const int f0 = min(96 * j, FF - 96);
  const int m2 = xcd * 4 + (slot >> 3), n2 = slot & 7;
  const int i16 = lane & 15, g = lane >> 4;
  char* ws = a.ws;
  const int rot2 = ((n2 * 11) / 4 + ((slot >> 3) * 11) / 2 + xcd * 3) % 22;
  const unsigned vd0 = (lane >> 3) * 1024 + (((lane & 7) ^ ((g & 1) << 2)) * 16);
  const unsigned pa_c = paddr(lbase, lane, 0, 3);

  if (w < 4) {
    const int tg = w & 1, fh = w >> 1;
    const int r = lane & 31, h = lane >> 5;
    const unsigned one2 = ((i16 & 7) == 4 * (g & 1)) ? 0x3F803F80u : 0u;
    v4u sel = {one2, one2, one2, one2};
    int xo[4], wo[2];
#pragma unroll
    for (int p = 0; p < 4; ++p) xo[p] = (tg * 64 + r) * 128 + (((2 * (p ^ (2 * tg)) + h) ^ ((r >> 1) & 7)) * 16);
    wo[0] = WGO + (g & 1) * 12320 + (8 * h + (i16 >> 2)) * 192 + fh * 96 + (lane & 3) * 8;
    wo[1] = wo[0] + (1 - tg) * 6144;
    wo[0] += tg * 6144;
    int zrow;
    {
      const int e = (r & 3) + 4 * (r >> 3), hh = (r >> 2) & 1;
      const unsigned long long lo = hh ? 0x1019181818170909ull : 0x0706050403020100ull;
      const unsigned long long hi = hh ? 0x160F150E121B0B10ull : 0x140D130C111A0A08ull;
      zrow = (int)(((e < 8 ? lo : hi) >> (8 * (e & 7))) & 31);
    }
    int zc_ = L_ZT + w * ZTS + zrow * 272 + h * 16, zf_ = L_ZT + w * ZTS + r * 272 + h * 16;
    const unsigned pa_l = lbase + L_PROG + 16;
#pragma unroll
    for (int p = 0; p < 4; ++p) PIN(xo[p]);
    PIN(wo[0]); PIN(wo[1]); PIN(zc_); PIN(zf_); PIN(sel);
    waitv<0>();
#pragma unroll
    for (int e = 0; e < 7; ++e) PIN(pq[e]);
    PIN(codetouch); PIN(epoch_v);
    const unsigned tag = __builtin_amdgcn_readfirstlane(epoch_v) * 0x9E3779B1u + 0x7F4A7C15u;
    {
      const float dt = rbf(expf(bf2f(pq[6])));
      const float ar = (dt * bf2f(pq[0])) * 1.44269504088896341f;
      float ai = (dt * bf2f(pq[1])) * 0.15915494309189535f;
      ai -= __builtin_rintf(ai);
      const float br = dt * bf2f(pq[2]), bi = dt * bf2f(pq[3]), cr0 = bf2f(pq[4]), ci0 = bf2f(pq[5]);
      f32x2 C = {cr0 * br - ci0 * bi, -(cr0 * bi + ci0 * br)};
      const f32x2 zc = f32x2{__builtin_amdgcn_cosf(ai), -__builtin_amdgcn_sinf(ai)} * __builtin_amdgcn_exp2f(ar);
      const f32x2 u8 = f32x2{__builtin_amdgcn_cosf(ai * 8.f), __builtin_amdgcn_sinf(ai * 8.f)} * __builtin_amdgcn_exp2f(ar * 8.f);
      f32x2 b0 = f32x2{__builtin_amdgcn_cosf(ai * 128.f), __builtin_amdgcn_sinf(ai * 128.f)} * __builtin_amdgcn_exp2f(ar * 128.f);
      const f32x2 u8c = {u8.x, -u8.y}, b0c = {b0.x, -b0.y};
      LAS unsigned* zt = (LAS unsigned*)(L + L_ZT + w * ZTS + lane * 4);
      f32x2 vc, E, b1, u16_, D = C, ex = C;
      cm4(vc, E, b1, u16_, b0c, b0c, C, u8c, b0, u8, u8, u8);
      zt[0] = pk2v(C); zt[8 * 68] = pk2v(E);
      f32x2 c6 = C;
#pragma unroll
      for (int k = 1; k < 8; ++k) {
        if (k == 3 || k == 5) ex = C;
        if (k == 7) c6 = C;
        cm4(C, D, E, ex, C, zc, D, vc, E, vc, ex, vc);
        zt[k * 68] = pk2v(C); zt[(8 + k) * 68] = pk2v(D); zt[(15 + k) * 68] = pk2v(E);
        if (k == 3) zt[23 * 68] = pk2v(ex);
        if (k == 5) zt[24 * 68] = pk2v(ex);
        if (k == 6) zt[26 * 68] = pk2v(ex);
        if (k == 7) zt[27 * 68] = pk2v(ex);
      }
      f32x2 a0 = u16_, a1, b2, b3;
      cm4(c6, a1, b2, b3, c6, vc, u8, u16_, b0, u16_, b1, u16_);
      zt[25 * 68] = pk2v(c6);
      asm volatile("" ::: "memory");
      v4u fc[8], ff[8];
#pragma unroll
      for (int ks = 0; ks < 8; ++ks) fc[ks] = *(LAS v4u*)(L + zc_ + ks * 32);
      asm volatile("" ::: "memory");
      zt[0] = 0x00003F80u; zt[68] = pk2v(u8); zt[16 * 68] = pk2v(b0); zt[17 * 68] = pk2v(b1);
      zt[2 * 68] = pk2v(a0); zt[3 * 68] = pk2v(a1); zt[18 * 68] = pk2v(b2); zt[19 * 68] = pk2v(b3);
#pragma unroll
      for (int k = 2; k < 8; ++k) {
        cm4(a0, a1, b2, b3, a0, u16_, a1, u16_, b2, u16_, b3, u16_);
        zt[2 * k * 68] = pk2v(a0); zt[(2 * k + 1) * 68] = pk2v(a1); zt[(16 + 2 * k) * 68] = pk2v(b2); zt[(17 + 2 * k) * 68] = pk2v(b3);
      }
      asm volatile("" ::: "memory");
#pragma unroll
      for (int ks = 0; ks < 8; ++ks) ff[ks] = *(LAS v4u*)(L + zf_ + ks * 32);
      sched0();
      f32x16 kd = {};
#pragma unroll
      for (int ks = 0; ks < 8; ++ks) kd = mfma32(fc[ks], ff[ks], kd);
#pragma unroll
      for (int g4 = 0; g4 < 4; ++g4) *(LAS f32x4*)(L + part_off(w) + g4 * 1024 + lane * 16) = f32x4{kd[4 * g4], kd[4 * g4 + 1], kd[4 * g4 + 2], kd[4 * g4 + 3]};
      asm volatile("" ::: "memory");
      prog_set(L, w, 1u);
    }
    if (2 * j - fh < 28) {
      f32x16 acc[2][3];
#pragma unroll
      for (int i = 0; i < 2; ++i)
#pragma unroll
        for (int c = 0; c < 3; ++c) acc[i][c] = f32x16{};
      f32x4 cs[3] = {};
      v4u xf[2][2], wf[2][3];
      auto ldf = [&](int buf, int sl, int p) {
        LAS char* S = L + sl * RS;
        LAS char* Wp = S + wo[p >> 1] + (p & 1) * 3072;
        wf[buf][0] = trd2(Wp, Wp + 768);
        xf[buf][0] = *(LAS v4u*)(S + xo[p]);
        xf[buf][1] = *(LAS v4u*)(S + xo[p] + 4096);
        wf[buf][1] = trd2(Wp + 32, Wp + 800);
        wf[buf][2] = trd2(Wp + 64, Wp + 832);
      };
      auto mmc = [&](int buf, int c) { acc[0][c] = mfma32(wf[buf][c], xf[buf][0], acc[0][c]); acc[1][c] = mfma32(wf[buf][c], xf[buf][1], acc[1][c]); };
      auto mm = [&](int buf, bool dosel) {
        mmc(buf, 0); mmc(buf, 1); mmc(buf, 2);
        if (dosel) {
#pragma unroll
          for (int c = 0; c < 3; ++c) cs[c] = mfma16(sel, wf[buf][c], cs[c]);
        }
      };
      auto pattern = [&]() { SGB(0x8, 1); SGB(0x100, 2); SGB(0x8, 1); SGB(0x100, 2); SGB(0x8, 1); SGB(0x100, 2); SGB(0x8, 1); SGB(0x100, 3); SGB(0x8, 5); sched0(); };
      spin(pa_l, 1u);
      ldf(0, 0, 0);
      sched0();
      unsigned pw = lbase + L_PROG + w * 4;
      unsigned pl[2] = {pa_l, pa_l + 4};
      PIN(pw); PIN(pl[0]); PIN(pl[1]);
#pragma unroll
      for (int s = 0; s < 7; ++s) {
        const int sl = s % 3;
        ldf(1, sl, 1); mm(0, true); pattern();
        ldf(0, sl, 2);
        const unsigned full = peek(pl[(s + 1) & 1]);
        unsigned pv = s + 2;
        PIN(pv);
        mm(1, true); pattern();
        ldf(1, sl, 3);
        unsigned fs = __builtin_amdgcn_readfirstlane(full);
        mmc(0, 0); mmc(0, 1);
        SGB(0x8, 1); SGB(0x100, 2); SGB(0x2, 1); SGB(0x8, 1); SGB(0x100, 2); SGB(0x8, 1); SGB(0x100, 2); SGB(0x8, 1); SGB(0x100, 2);
        sched0();
        if (s == 6) {
#pragma unroll
          for (int c = 0; c < 3; ++c) *(LAS float*)(L + L_CSUM + w * 768 + c * 256 + lane * 4) = cs[c][0];
          asm volatile("" ::: "memory");
        }
        put1(pw, pv);
        sched0();
        mmc(0, 2);
        sched0();
        spin_if(fs, pl[(s + 1) & 1], s + 2);
        asm volatile("" ::: "memory");
        sched0();
        ldf(0, (s + 1) % 3, 0); mm(1, false); pattern();
      }
      const unsigned pq_ = peek(lbase + L_PROG + 24);
      ldf(1, 1, 1);
      const unsigned pp_ = peek(lbase + L_PROG + (w ^ 1) * 4);
      mmc(0, 0); mmc(0, 1);
      SGB(0x8, 1); SGB(0x100, 3); SGB(0x8, 1); SGB(0x100, 3); SGB(0x8, 1); SGB(0x100, 2); SGB(0x8, 1); SGB(0x100, 2); sched0();
      const unsigned ws_ = __builtin_amdgcn_readfirstlane(pq_);
      mmc(0, 2);
#pragma unroll
      for (int c = 0; c < 3; ++c) cs[c] = mfma16(sel, wf[0][c], cs[c]);
      sched0();
      spin_if(ws_, lbase + L_PROG + 24, 3u);
      sched0();
      const unsigned sv[2] = {*(LAS unsigned*)(L + L_STAT + (tg * 64 + r) * 4), *(LAS unsigned*)(L + L_STAT + (tg * 64 + 32 + r) * 4)};
      ldf(0, 1, 2);
      const unsigned ps_ = __builtin_amdgcn_readfirstlane(pp_);
      float c1[2], ir2[2];
      v4u xa[2], wa[3];
#pragma unroll
      for (int i = 0; i < 2; ++i) {
        const unsigned nm = h ? 0u : (sv[i] & 0xffffu) ^ 0x8000u;
        xa[i] = v4u{nm | (nm << 16), nm, 0u, 0u};
        const float sd = bfhi(sv[i]);
        c1[i] = cut(-1.44269504088896341f * __builtin_amdgcn_rcpf(sd)); ir2[i] = cut(sd * sd);
        asm("" : "+v"(xa[i]));
      }
      mm(1, true);
      SGB(0x8, 1); SGB(0x100, 3); SGB(0x2, 2); SGB(0x8, 1); SGB(0x100, 3); SGB(0x8, 1); SGB(0x100, 2); SGB(0x8, 1); SGB(0x100, 2); SGB(0x2, 3);
      SGB(0x8, 1); SGB(0x2, 5); SGB(0x8, 1); SGB(0x2, 5); SGB(0x8, 1); SGB(0x2, 2); SGB(0x8, 1); SGB(0x2, 2); SGB(0x8, 1); sched0();
      spin_if(ps_, lbase + L_PROG + (w ^ 1) * 4, 8u);
      asm volatile("" ::: "memory");
      sched0();
      ldf(1, 1, 3);
      float csp[3];
#pragma unroll
      for (int c = 0; c < 3; ++c) csp[c] = *(LAS float*)(L + L_CSUM + (w ^ 1) * 768 + c * 256 + lane * 4);
      mmc(0, 0); mmc(0, 1);
      SGB(0x8, 1); SGB(0x100, 3); SGB(0x8, 1); SGB(0x100, 3); SGB(0x8, 1); SGB(0x100, 3); SGB(0x8, 1); SGB(0x100, 2); sched0();
      __builtin_amdgcn_s_setprio(1);
      put1(pw, 10u);
      sched0();
      mmc(0, 2);
#pragma unroll
      for (int c = 0; c < 3; ++c) cs[c] = mfma16(sel, wf[0][c], cs[c]);
#pragma unroll
      for (int c = 0; c < 3; ++c) cs[c] = mfma16(sel, wf[1][c], cs[c]);
      sched0();
      mmc(1, 0);
      sched0();
#pragma unroll
      for (int c = 0; c < 3; ++c) {
        const float s = cut(cs[c][0] + csp[c]);
        const float hi = bfhi(__builtin_bit_cast(unsigned, s)), d1 = cut(s - hi), mid = bfhi(__builtin_bit_cast(unsigned, d1)), lo = cut(d1 - mid);
        wa[c] = v4u{__builtin_amdgcn_perm(__builtin_bit_cast(unsigned, d1), __builtin_bit_cast(unsigned, s), 0x07060302u), __builtin_bit_cast(unsigned, lo) >> 16, 0u, 0u};
      }
      mmc(1, 1); mmc(1, 2);
      SGB(0x2, 5); SGB(0x8, 1); SGB(0x2, 6); SGB(0x8, 1); SGB(0x2, 6); SGB(0x8, 1); SGB(0x2, 6); SGB(0x8, 1); sched0();
#pragma unroll
      for (int c = 0; c < 3; ++c) { acc[0][c] = mfma32(wa[c], xa[0], acc[0][c]); acc[1][c] = mfma32(wa[c], xa[1], acc[1][c]); }
      const __amdgpu_buffer_rsrc_t rhs = __builtin_amdgcn_make_buffer_rsrc((void*)(ws + WS_H), 0, H_BYTES, 0x00020000);
#pragma unroll
      for (int c = 0; c < 3; ++c) {
        const int F16 = f0 / 16 + fh * 3 + c;
#pragma unroll
        for (int i = 0; i < 2; ++i) {
          float hv[8];
#pragma unroll
          for (int e = 0; e < 8; ++e) {
            const float gv = acc[i][c][e], u = acc[i][c][e + 8];
            const float ex = __builtin_amdgcn_exp2f(gv * c1[i]);
            hv[e] = (gv * u) * __builtin_amdgcn_rcpf(ex * ir2[i] + ir2[i]);
          }
          const v4u o = {pk2(hv[0], hv[1]), pk2(hv[2], hv[3]), pk2(hv[4], hv[5]), pk2(hv[6], hv[7])};
          const int so = (((m * 4 + tg * 2 + i) * 86 + F16) * 1024) | (((F16 - 6 * j) >> 31) & 0x40000000);
          __builtin_amdgcn_raw_buffer_store_b128(o, rhs, lane * 16, so, 16);
          asm volatile("s_nop 1");
          sched0();
        }
      }
    }
    prog_set(L, w, 12u);
    const int cl = w >> 1, rh_ = w & 1;
    f32x4 y[8];
#pragma unroll
    for (int t = 0; t < 8; ++t) y[t] = f32x4{};
    {
      const int s0 = 2031 - i16 + 8 * g - rh_ * 128 - 96;
      LAS char* ka = L + L_KIMG + cl * 2 * CS + (s0 & 1) * CS + ((s0 + 1) >> 1) * 4;
      LAS char* ub = L + L_UIMG + cl * US + 512 + (16 * i16 - 16 + 8 * g - rh_ * 128 - 96) * 2;
      LAS char* kq_[4];
#pragma unroll
      for (int q = 0; q < 4; ++q) { kq_[q] = ka - (2 * q + 1) * 512; asm volatile("" : "+v"(kq_[q])); }
      v4u fa[2][8], fb[2][8];
      auto ldc = [&](int buf, int rr_) {
#pragma unroll
        for (int q = 0; q < 8; ++q) {
          LAS unsigned* p = (LAS unsigned*)(kq_[q >> 1] + ((q & 1) ? 0 : 512) + (3 - rr_) * 64);
          fa[buf][q] = v4u{p[0], p[1], p[2], p[3]};
          fb[buf][q] = *(LAS v4u*)(ub + q * 512 + (3 - rr_) * 64);
        }
      };
      auto publish = [&]() {
        waitv<0>();
        if (j < 15) flag1(ws + WS_F2, (unsigned)(((m * 2 + tg) * 16 + j) * 2 + fh) * 128, tag);
        prog_set(L, w, 13u);
      };
      const unsigned pa_w = paddr(lbase, lane, 6, 1);
      bool early = true;
      if (__any(peek(pa_w) < 5u)) { publish(); early = false; spin_slow(pa_w, 5u); }
      ldc(0, 0);
      sched0();
      if (early) publish();
      sched0();
#pragma unroll
      for (int rr_ = 0; rr_ < 4; ++rr_) {
        if (rr_ < 3) ldc((rr_ + 1) & 1, rr_ + 1);
#pragma unroll
        for (int d = 0; d < 8; ++d)
#pragma unroll
          for (int o = 0; o <= d; ++o) {
            if (d + o < 8) y[d + o] = mfma16(fa[rr_ & 1][d], fb[rr_ & 1][o], y[d + o]);
            if (o < d && d + o < 8) y[d + o] = mfma16(fa[rr_ & 1][o], fb[rr_ & 1][d], y[d + o]);
          }
        if (rr_ < 3) {
#pragma unroll
          for (int k = 0; k < 24; ++k) { SGB(0x8, 1); SGB(0x100, 1); }
        }
        sched0();
        if (rr_ == 2) { asm volatile("" ::: "memory"); prog_set(L, w, 20u); }
        sched0();
      }
    }
    LAS char* xo_ = L + L_KIMG + cl * 2 * CS + rh_ * 4096;
    LAS char* xi_ = L + L_KIMG + cl * 2 * CS + (rh_ ^ 1) * 4096;
    const unsigned pa_p = lbase + L_PROG + (w ^ 1) * 4;
    const unsigned yo = (unsigned)((((rh_ * 16 + (i16 >> 2)) * 8 + (b >> 5)) * 64 + ((2 * b + cl) & 63)) * 256 + g * 64 + (i16 & 3) * 16);
    auto finish = [&](auto keep) {
      constexpr int K = decltype(keep)::value;
      spin(pa_p, 20u);
#pragma unroll
      for (int t = 0; t < 4; ++t) *(LAS f32x4*)(xo_ + t * 1024 + lane * 16) = y[(K ^ 4) + t];
      asm volatile("" ::: "memory");
      prog_set(L, w, 21u);
      spin(pa_p, 21u);
      f32x4 yp[4];
#pragma unroll
      for (int t = 0; t < 4; ++t) yp[t] = *(LAS f32x4*)(xi_ + t * 1024 + lane * 16);
#pragma unroll
      for (int t = 0; t < 4; ++t) {
        const f32x4 o = yp[t];
        const v4u v = {pk2s(y[K + t][0] + o[0], y[K + t][1] + o[1]), tag, pk2s(y[K + t][2] + o[2], y[K + t][3] + o[3]), tag};
        st16c(ws + WS_YT + t * (4 * 8 * 64 * 256), yo, v);
      }
    };
    if (rh_) finish(IC<4>{}); else finish(IC<0>{});
    __builtin_amdgcn_s_setprio(0);
    spin_slow(lbase + L_PROG + (4 + w) * 4, 14u);
    v4u a0, a1, a2, a3;
    {
      const char* yb = ws + WS_YT + (m2 * 8 + n2) * 16384 + hw * 4096;
#pragma unroll 1
      for (unsigned sp = 0; sp < SPIN_CAP; ++sp) {
        ld16c(a0, yb, lane * 16); ld16c(a1, yb + 1024, lane * 16); ld16c(a2, yb + 2048, lane * 16); ld16c(a3, yb + 3072, lane * 16);
        waitv<0>(); PIN(a0); PIN(a1); PIN(a2); PIN(a3);
        if (__all(a0.y == tag && a0.w == tag && a1.y == tag && a1.w == tag && a2.y == tag && a2.w == tag && a3.y == tag && a3.w == tag)) break;
        __builtin_amdgcn_s_sleep(6);
      }
    }
    {
      LAS char* p = xi_ + g * 128 + ((i16 & 3) * 16 + (i16 >> 2) * 4) * 2;
      *(LAS v2u*)p = v2u{a0.x, a0.z}; *(LAS v2u*)(p + 512) = v2u{a1.x, a1.z}; *(LAS v2u*)(p + 1024) = v2u{a2.x, a2.z}; *(LAS v2u*)(p + 1536) = v2u{a3.x, a3.z};
    }
    {
      v4u av[2], xv[2];
#pragma unroll
      for (int it = 0; it < 2; ++it) {
        const int c8 = hw * 2 + it;
        LAS char* p = xi_ + (it * 8 + (i16 >> 2)) * 128 + (g * 16 + (i16 & 3) * 4) * 2;
        av[it] = trd2(p, p + 512);
        xv[it] = *(LAS v4u*)(L + L_BASE + lane * 128 + ((c8 ^ (lane & 7)) * 16));
      }
#pragma unroll
      for (int it = 0; it < 2; ++it) {
        v4u o;
#pragma unroll
        for (int e = 0; e < 4; ++e) o[e] = pk2s(bflo(xv[it][e]) + bflo(av[it][e]), bfhi(xv[it][e]) + bfhi(av[it][e]));
        *(LAS v4u*)(L + L_BASE + lane * 128 + (((hw * 2 + it) ^ (lane & 7)) * 16)) = o;
      }
    }
    asm volatile(".Lfk_e2:");
  } else {
    const unsigned pa_w = paddr(lbase, lane, 6, 1);
    const int kq = hw, r = lane & 31, h = lane >> 5;
    LAS char* W = L + kq * 2048;
    const int fb = min(22 * kq, 64);
    const int wa0 = (4 * h + (i16 >> 2)) * 128 + (i16 >> 3) * 64 + (g & 1) * 32 + (lane & 3) * 8;
    const int wa1 = wa0 ^ 64;
    const char* wds = (const char*)a.wd + (size_t)fb * 16384 + n2 * 128;
    int wi = rot2 + 5;
    wi = wi >= 22 ? wi - 22 : wi;
    auto wd_steps = [&](int n, int pos) {
#pragma unroll 1
      for (int i = 0; i < n; ++i) {
        dma2(wds + (size_t)wi * 16384, vd0, vd0 + 7168, lbase + pos + kq * 2048);
        wi = wi == 21 ? 0 : wi + 1;
        pos += 8192;
        waitv<WWIN>();
      }
    };
    if (w >= 6) {
      const int wk = w - 6, wt = tid - 384;
      waitv<0>();
#pragma unroll
      for (int e = 0; e < 8; ++e) PIN(rr[e]);
      PIN(epoch_w); PIN(dsk_v);
      const unsigned epoch = __builtin_amdgcn_readfirstlane(epoch_w);
      const unsigned tag = epoch * 0x9E3779B1u + 0x7F4A7C15u;
      prog_set(L, 8, tag);
      v4u on[4];
      {
        float inv[2], nb[2];
#pragma unroll
        for (int q = 0; q < 2; ++q) {
          float s0 = 0.f, s1 = 0.f, q0 = 0.f, q1 = 0.f;
#pragma unroll
          for (int i = 0; i < 4; ++i) {
            s0 = dot2(rr[4 * q][i], 0x3F803F80u, s0); q0 = dot2(rr[4 * q][i], rr[4 * q][i], q0);
            s1 = dot2(rr[4 * q + 1][i], 0x3F803F80u, s1); q1 = dot2(rr[4 * q + 1][i], rr[4 * q + 1][i], q1);
            s0 = dot2(rr[4 * q + 2][i], 0x3F803F80u, s0); q0 = dot2(rr[4 * q + 2][i], rr[4 * q + 2][i], q0);
            s1 = dot2(rr[4 * q + 3][i], 0x3F803F80u, s1); q1 = dot2(rr[4 * q + 3][i], rr[4 * q + 3][i], q1);
          }
          const float mean = rsum16(cut(s0 + s1)) * (1.f / DM);
          const float var = fmaxf(rsum16(cut(q0 + q1)) * (1.f / DM) - mean * mean, 0.f);
          const float mu = rbf(mean), sd = rbf(__builtin_amdgcn_sqrtf(rbf(rbf(var) + 1e-5f)));
          inv[q] = __builtin_amdgcn_rcpf(sd); nb[q] = -mu * inv[q];
          if (i16 == 0 && !(jl & 1)) st8c(ws + WS_STAT, (unsigned)(t0 + 4 * wk + g + 8 * q) * 8, v2u{pk2(mu, sd), tag});
        }
#pragma unroll
        for (int k = 0; k < 2; ++k) {
          unsigned o[2][4];
#pragma unroll
          for (int q = 0; q < 2; ++q)
#pragma unroll
            for (int i = 0; i < 4; ++i) o[q][i] = pk2s(bflo(rr[4 * q + k][i]) * inv[q] + nb[q], bfhi(rr[4 * q + k][i]) * inv[q] + nb[q]);
          on[2 * k] = v4u{o[0][0], o[1][0], o[0][1], o[1][1]};
          on[2 * k + 1] = v4u{o[0][2], o[1][2], o[0][3], o[1][3]};
          PIN(on[2 * k]); PIN(on[2 * k + 1]);
        }
      }
      spin_slow(pa_c, 1u);
      f32x4 pt[2][4];
      {
        LAS char* p = L + L_UIMG + (4 * wk + g) * LNS + i16 * 32;
        *(LAS v4u*)p = on[0]; *(LAS v4u*)(p + 16) = on[1]; *(LAS v4u*)(p + 512) = on[2]; *(LAS v4u*)(p + 528) = on[3];
#pragma unroll
        for (int g4 = 0; g4 < 4; ++g4) { pt[0][g4] = *(LAS f32x4*)(L + part_off(2 * wk) + g4 * 1024 + lane * 16); pt[1][g4] = *(LAS f32x4*)(L + part_off(2 * wk + 1) + g4 * 1024 + lane * 16); }
      }
      asm volatile("" ::: "memory");
      prog_set(L, w, 1u);
      spin(pa_w, 1u);
      {
        LAS char* xp = L + L_UIMG + (i16 >> 2) * LNS + (wk * 64 + g * 4 + (lane & 3)) * 8;
        const unsigned xto = (unsigned)(((jl & 1) * 128 + wk * 64 + g * 4 + (i16 >> 2)) * 8192 + (t0 >> 4) * 64 + (lane & 3) * 16);
        v4u xv[4];
#pragma unroll
        for (int it = 0; it < 4; ++it) xv[it] = trd2(xp + it * 128, xp + it * 128 + 4 * LNS);
#pragma unroll
        for (int it = 0; it < 4; ++it) st16c(ws + WS_XT + it * 16 * 8192, xto, xv[it]);
      }
      {
        float v[16];
#pragma unroll
        for (int e = 0; e < 16; ++e) v[e] = cut(pt[0][e >> 2][e & 3] + pt[1][e >> 2][e & 3]);
        if (lane == 0) v[0] += bf2f(dsk_v);
        const int B = lane & 31, hh = lane >> 5;
        LAS char* k0 = L + L_KIMG + wk * 2 * CS + (255 - B) * 16;
        LAS char* ka = k0 - hh * 512;
        *(LAS v4u*)ka = v4u{pk2(v[7], v[6]), pk2(v[5], v[4]), pk2(v[3], v[2]), pk2(v[1], v[0])};
        *(LAS v4u*)(ka + CS) = v4u{pk2(v[8], v[7]), pk2(v[6], v[5]), pk2(v[4], v[3]), pk2(v[2], v[1])};
        const unsigned p00 = pk2(v[9], v[9]), p44 = pk2(v[10], v[10]), p88 = pk2(v[11], v[11]);
        *(LAS v4u*)(ka - 1024) = v4u{p88, p44, pk2(v[10], v[9]), p00};
        *(LAS v4u*)(ka - 1024 + CS) = v4u{p88, pk2(v[11], v[10]), p44, p00};
        LAS char* kc = k0 - 2048 - hh * 1024;
#pragma unroll
        for (int t = 0; t < 2; ++t) {
          const float lo = v[12 + 2 * t], hi = v[13 + 2 * t], md = (B & 1) ? hi : lo;
          const unsigned ll = pk2(lo, lo), hh2 = pk2(hi, hi);
          *(LAS v4u*)(kc - t * 512) = v4u{hh2, pk2(hi, md), ll, ll};
          *(LAS v4u*)(kc - t * 512 + CS) = v4u{hh2, hh2, pk2(md, lo), ll};
        }
        if (lane < 4) {
          *(LAS v4u*)(L + L_KIMG + wk * 2 * CS + 4096 + lane * 16) = v4u{0u, 0u, 0u, 0u};
          *(LAS v4u*)(L + L_KIMG + wk * 2 * CS + CS + 4096 + lane * 16) = v4u{lane == 0 ? pk2(v[0], 0.f) : 0u, 0u, 0u, 0u};
        }
      }
      waitv<0>();
      prog_done(L, w, 2u);
      if (__all(peek(pa_w) >= 2u)) { if (lane == 0) st4c(ws + WS_F1, (unsigned)b * 4, tag); }
      if (j == 15) { for (int i = 0; i < 6; ++i) __builtin_amdgcn_s_sleep(16); }
      else spin_slow(lbase + L_PROG + 16, 7u);
      v4u pfl, pst;
      ld16c(pst, ws + WS_STAT + m * 1024, lane * 16); ld16c(pfl, ws + WS_F1, lane * 16);
      waitv<0>(); PIN(pst); PIN(pfl);
      if (wk == 0) {
#pragma unroll 1
        for (unsigned sp = 0; sp < SPIN_CAP; ++sp) {
          if (__all(pst.y == tag && pst.w == tag)) break;
          __builtin_amdgcn_s_sleep(8);
          ld16c(pst, ws + WS_STAT + m * 1024, lane * 16); waitv<0>(); PIN(pst);
        }
        *(LAS v2u*)(L + L_STAT + lane * 8) = v2u{pst.x, pst.z};
        asm volatile("" ::: "memory");
        prog_set(L, w, 3u);
        asm volatile(".Lfk_e3:");
      }
#pragma unroll 1
      for (unsigned sp = 0; sp < SPIN_CAP; ++sp) {
        if (__all(pfl.x == tag && pfl.y == tag && pfl.z == tag && pfl.w == tag)) break;
        __builtin_amdgcn_s_sleep(8);
        ld16c(pfl, ws + WS_F1, lane * 16); waitv<0>(); PIN(pfl);
      }
      v4u u0, u1, u2, u3;
      const char* xb = ws + WS_XT + b * 8192;
      ld16c(u0, xb, wt * 16); ld16c(u1, xb + 2048, wt * 16); ld16c(u2, xb + 4096, wt * 16); ld16c(u3, xb + 6144, wt * 16);
      {
        const unsigned vo = (unsigned)((lane >> 3) * 1024 + (lane & 7) * 16);
        const char* xt = (const char*)a.x + (size_t)(m2 * 64 + wk * 32) * 1024 + n2 * 128;
        ld16(rr[0], xt, vo); ld16(rr[1], xt + 8192, vo); ld16(rr[2], xt + 16384, vo); ld16(rr[3], xt + 24576, vo);
      }
      if (b == 0 && wt == 0) st4c(ws + WS_EPOCH, 0u, epoch + 1u);
      LAS char* up = L + L_UIMG + (wt & 1) * US + 512 + ((wt >> 2) * 16 + ((wt >> 1) & 1) * 8) * 2;
      if (lane < 32) *(LAS v4u*)(L + L_UIMG + wk * US + lane * 16) = v4u{0u, 0u, 0u, 0u};
      waitv<0>(); PIN(u0); PIN(u1); PIN(u2); PIN(u3);
      *(LAS v4u*)up = u0; *(LAS v4u*)(up + 1024) = u1; *(LAS v4u*)(up + 2048) = u2; *(LAS v4u*)(up + 3072) = u3;
#pragma unroll
      for (int e = 0; e < 4; ++e) { PIN(rr[e]); *(LAS v4u*)(L + L_BASE + wk * 4096 + e * 1024 + (lane >> 3) * 128 + (((lane & 7) ^ (lane >> 3)) * 16)) = rr[e]; }
      asm volatile("" ::: "memory");
      prog_set(L, w, 5u);
      asm volatile(".Lfk_e1:");
    } else {
      __builtin_amdgcn_s_setprio(1);
      unsigned vx[8], vw[12];
      {
        const int r = lane >> 3, cp = lane & 7;
#pragma unroll
        for (int i = 0; i < 12; ++i) { const int n = (i % 3) * 64 + lane; vw[i] = ((i / 3) * 16 + n / 12) * (FF * 2) + (n % 12) * 16 - (i & 3) * 1024; PIN(vw[i]); }
#pragma unroll
        for (int i = 0; i < 8; ++i) { vx[i] = r * 1024 + ((cp ^ ((i & 1) * 4 + g)) * 16) + i * 8192 - (i & 3) * 1024; PIN(vx[i]); }
      }
      const int rot1 = (m + 2 * j) & 7;
      const char* xs0 = (const char*)a.x + (size_t)(m * 128) * 1024;
      const char* gs0 = (const char*)a.wg + f0 * 2;
      const char* us0 = (const char*)a.wu + f0 * 2;
      auto stage = [&](auto pace, int s, int sl) {
        constexpr int P = decltype(pace)::value;
        const unsigned lb = lbase + sl * RS;
        const int ks = (s + rot1) & 7;
        dma12<P>(gs0 + (size_t)ks * (64 * FF * 2), vw, lb + WGO);
        dma8<P>(xs0 + ks * 128, vx, lb);
        dma12<P>(us0 + (size_t)ks * (64 * FF * 2), vw, lb + WUO);
        dma8<P>(xs0 + ks * 128 + 65536, vx, lb + 8192);
      };
      if (j < 15) {
        if (hw) spin(lbase + L_PROG + P_MARK, 1u);
        stage(IC<0>{}, hw, hw);
        if (!hw) put1(lbase + L_PROG + P_MARK, 1u);
        waitv<0>();
        prog_set(L, w, hw + 1);
        int sl = hw ? 0 : 2;
#pragma unroll 1
        for (int it = 1; it < 4; ++it) {
          const int s = 2 * it + hw;
          if (s >= 3) spin(pa_c, s - 1);
          stage(IC<PACE>{}, s, sl);
          sl = sl == 0 ? 2 : sl - 1;
          if (s == 7) put1(lbase + L_PROG + P_MARK, 2u);
          waitv<0>();
          prog_set(L, w, s + 1);
        }
      }
      if (!hw) {
        unsigned vd[8];
#pragma unroll
        for (int i = 0; i < 8; ++i) vd[i] = vd0 + (unsigned)(min(22 * (i >> 1), 64) * 16384 + (i & 1) * 8192 - (i & 3) * 1024);
        if (j < 15) { spin(lbase + L_PROG + P_MARK, 2u); __builtin_amdgcn_s_sleep(3); }
        spin(pa_c, 7u);
        int ii = rot2;
        {
          const int i6 = rot2 + 5 >= 22 ? rot2 - 17 : rot2 + 5;
          touch((const char*)a.wd + (size_t)i6 * 16384 + n2 * 128, (unsigned)(min(22 * g, 64) * 16 + i16) * 1024, lbase + L_CSUM + 3072);
        }
#pragma unroll 1
        for (int i = 0; i < 5; ++i) {
          ii = ii >= 22 ? ii - 22 : ii;
          dma8<0>((const char*)a.wd + (size_t)ii * 16384 + n2 * 128, vd, lbase + 2 * RS + i * 8192);
          ++ii;
        }
      }
      __builtin_amdgcn_s_setprio(0);
    }
    LAS char* wp[3][2];
#pragma unroll
    for (int k = 0; k < 3; ++k) { wp[k][0] = W + wd_pos(5 * k) + wa0; wp[k][1] = W + wd_pos(5 * k) + wa1; PIN(wp[k][0]); PIN(wp[k][1]); }
    auto wfrag = [&](v4u* d, int i) {
      d[0] = trd2(wp[i / 5][0] + (i % 5) * 8192, wp[i / 5][0] + (i % 5) * 8192 + 1024); d[1] = trd2(wp[i / 5][1] + (i % 5) * 8192, wp[i / 5][1] + (i % 5) * 8192 + 1024);
    };
    LAS char* pw_[8];
#pragma unroll
    for (int k = 0; k < 8; ++k) { pw_[k] = W + (r >> 3) * 8192 + (r & 7) * 256 + (((2 * k + h) ^ (r & 15)) * 16); PIN(pw_[k]); }
    spin_slow(lbase + L_PROG + 24, 1u);
    unsigned tag = __builtin_amdgcn_readfirstlane(peek(lbase + L_PROG + P_TAG));
    unsigned fo = (unsigned)min((22 * kq) / 3 + lane, (22 * kq + 21) / 3) * 128;
    const char* fbase = ws + WS_F2 + m2 * 4096;
    PIN(fo); asm volatile("" : "+s"(tag), "+s"(fbase));
    __builtin_amdgcn_s_setprio(2);
    if (w == 4) { waitv<0>(); prog_set(L, w, 9u); } else spin_slow(lbase + L_PROG + 16, 9u);
    v4u we[7][2];
#pragma unroll
    for (int i = 0; i < 5; ++i) wfrag(we[i], i);
    spin(pa_c, 10u);
    wd_steps(5, 0);
    wd_steps(5, RS);
#pragma unroll
    for (int i = 5; i < 7; ++i) wfrag(we[i], i);
    wd_steps(4, 2 * RS);
    unsigned fv;
    ld4c(fv, fbase, fo);
    wd_steps(1, 2 * RS + 4 * 8192);
#pragma unroll
    for (int i = 0; i < 7; ++i) { PIN(we[i][0]); PIN(we[i][1]); }
    waitl();
    wd_steps(2, 0);
    waitv<4>(); PIN(fv);
    const bool ready = __all(fv == tag);
    const __amdgpu_buffer_rsrc_t rh = __builtin_amdgcn_make_buffer_rsrc((void*)(ws + WS_H), 0, H_BYTES, 0x00020000);
    if (j == 15) spin_slow(pa_c, 20u);
    if (!ready) poll_eq(fbase, fo, tag);
    __builtin_amdgcn_s_setprio(0);
    v4u hf[HW][2];
    auto ldh = [&](int i) {
      int ii = i + rot2; ii = ii >= 22 ? ii - 22 : ii;
      const int F16 = fb + ii;
      const int so = ((m2 * 2 * 86 + F16) * 1024) | (((F16 - 22 * kq) >> 31) & 0x40000000);
      hf[i % HW][0] = __builtin_amdgcn_raw_buffer_load_b128(rh, lane * 16, so, 16);
      hf[i % HW][1] = __builtin_amdgcn_raw_buffer_load_b128(rh, lane * 16, so + 86 * 1024, 16);
    };
#pragma unroll
    for (int i = 0; i < HW0; ++i) ldh(i);
    sched0();
    f32x16 o2[2][2];
#pragma unroll
    for (int i = 0; i < 4; ++i) o2[i >> 1][i & 1] = f32x16{};
    v4u wn[2][2];
    constexpr int NT = 22 - TAILN;
    int nld = HW0;
#pragma unroll
    for (int i = 0; i < NT; ++i) {
      if (i + 1 >= 7 && i + 1 < NT) {
        wfrag(wn[(i + 1) & 1], i + 1 < 15 ? i + 1 : i + 1 - 15);
      }
      if (i >= NT - TAILN) {
        const int k = i - (NT - TAILN);
        wfrag(we[k], NT + k - 15);
      }
      const v4u w0 = i < 7 ? we[i < 7 ? i : 0][0] : wn[i & 1][0], w1 = i < 7 ? we[i < 7 ? i : 0][1] : wn[i & 1][1];
      const v4u h0 = hf[i % HW][0], h1 = hf[i % HW][1];
      o2[0][0] = mfma32(w0, h0, o2[0][0]); o2[1][0] = mfma32(w1, h0, o2[1][0]);
      o2[0][1] = mfma32(w0, h1, o2[0][1]); o2[1][1] = mfma32(w1, h1, o2[1][1]);
#pragma unroll
      for (int k = 0; k < 2; ++k) if (nld < 22 && nld < i + HW) { ldh(nld); ++nld; }
      if (i == YAT) prog_set(L, w, 14u);
      SGB(0x8, 1); SGB(0x100, 2); SGB(0x20, 1); SGB(0x8, 1); SGB(0x100, 2); SGB(0x20, 1); SGB(0x8, 1); SGB(0x100, 2); SGB(0x20, 1); SGB(0x8, 1); SGB(0x100, 2); SGB(0x20, 1);
      sched0();
    }
    asm volatile("" ::: "memory");
    auto wr = [&](int cc, int tt) {
#pragma unroll
      for (int g4 = 0; g4 < 4; ++g4)
        *(LAS f32x4*)(pw_[cc * 4 + g4] + tt * RS) = f32x4{o2[cc][tt][4 * g4], o2[cc][tt][4 * g4 + 1], o2[cc][tt][4 * g4 + 2], o2[cc][tt][4 * g4 + 3]};
    };
#pragma unroll
    for (int q = 0; q < 4; ++q) {
      const int cc = q & 1, tt = q >> 1;
#pragma unroll
      for (int k = 0; k < TAILN; ++k) o2[cc][tt] = mfma32(we[k][cc], hf[(NT + k) % HW][tt], o2[cc][tt]);
      if (q > 0) { wr((q - 1) & 1, (q - 1) >> 1); SGB(0x8, 1); SGB(0x200, 1); SGB(0x8, 1); SGB(0x200, 1); SGB(0x8, 1); SGB(0x200, 1); SGB(0x8, 1); SGB(0x200, 1); SGB(0x8, 2); }
      sched0();
    }
    wr(1, 1);
  }
  {
    const int tok = tid >> 3, c8 = tid & 7;
    LAS char* P = L + (tok >> 5) * RS + ((tok >> 3) & 3) * 8192 + (tok & 7) * 256;
    LAS char* p0 = P + (((2 * c8) ^ (tok & 15)) * 16);
    LAS char* p1 = P + (((2 * c8 + 1) ^ (tok & 15)) * 16);
    LAS char* pb = L + L_BASE + tok * 128 + ((c8 ^ (tok & 7)) * 16);
    unsigned oo = tok * 1024 + c8 * 16;
    char* ob = (char*)a.out + (size_t)(m2 * 64) * 1024 + n2 * 128;
    PIN(p0); PIN(p1); PIN(pb); PIN(oo); asm volatile("" : "+s"(ob));
    bar();
    asm volatile("" :: "s"(ka_touch));
    f32x4 q0[4], q1[4];
#pragma unroll
    for (int k = 0; k < 4; ++k) { q0[k] = *(LAS f32x4*)(p0 + k * 2048); q1[k] = *(LAS f32x4*)(p1 + k * 2048); }
    const v4u bv = *(LAS v4u*)pb;
    sched0();
    const f32x4 s0 = (q0[0] + q0[1]) + (q0[2] + q0[3]), s1 = (q1[0] + q1[1]) + (q1[2] + q1[3]);
    const unsigned f0_ = pk2(s0[0], s0[1]), f1_ = pk2(s0[2], s0[3]), f2_ = pk2(s1[0], s1[1]), f3_ = pk2(s1[2], s1[3]);
    const v4u o = {pk2(bflo(bv.x) + bflo(f0_), bfhi(bv.x) + bfhi(f0_)), pk2(bflo(bv.y) + bflo(f1_), bfhi(bv.y) + bfhi(f1_)),
                   pk2(bflo(bv.z) + bflo(f2_), bfhi(bv.z) + bfhi(f2_)), pk2(bflo(bv.w) + bflo(f3_), bfhi(bv.w) + bfhi(f3_))};
    asm volatile("global_store_dwordx4 %0, %1, %2 sc0 sc1\n\ts_nop 1" :: "v"(oo), "v"(o), "s"(ob) : "memory");
  }
  asm volatile(".Lfk_end:");
}

extern "C" void kernel_launch(void* const* d_in, const int* in_sizes, int n_in, void* d_out, int out_size, void* d_ws, size_t ws_size,
                              hipStream_t stream) {
  Args a;
  a.x = (const u16*)d_in[0]; a.are = (const u16*)d_in[1]; a.aim = (const u16*)d_in[2]; a.bre = (const u16*)d_in[3]; a.bim = (const u16*)d_in[4];
  a.cre = (const u16*)d_in[5]; a.cim = (const u16*)d_in[6]; a.logdt = (const u16*)d_in[7]; a.dsk = (const u16*)d_in[8];
  a.wg = (const u16*)d_in[10]; a.wu = (const u16*)d_in[11]; a.wd = (const u16*)d_in[12];
  a.out = (u16*)d_out; a.ws = (char*)d_ws;
  (void)hipFuncSetAttribute((const void*)fused_kernel, hipFuncAttributeMaxDynamicSharedMemorySize, L_TOTAL);
  fused_kernel<<<256, 512, L_TOTAL, stream>>>(a);
}
```
